```python
import torch

def _softmax(x, axis=-1):
    return torch.softmax(x, dim=axis)
def _gelu(x):
    return torch.nn.functional.gelu(x, approximate="tanh")
def _silu(x):
    return torch.nn.functional.silu(x)
def _sigmoid(x):
    return torch.sigmoid(x)
def _layernorm(x, axis=-1, eps=1e-5):
    m = x.mean(dim=axis, keepdim=True)
    v = x.var(dim=axis, keepdim=True, unbiased=False)
    return (x - m) / torch.sqrt(v + eps)
def _scatter_add(tgt, idx, src, axis=0):
    return tgt.clone().index_add_(axis, idx, src)
def _segment_sum(src, seg, n):
    out = torch.zeros((n,) + src.shape[1:], dtype=src.dtype)
    return out.index_add_(0, seg, src)
def _pad(x, pw):
    flat = tuple(v for pair in reversed(pw) for v in pair)
    return torch.nn.functional.pad(x, flat)

kernel_name = 'interleave_layers__spectral_gfnet__softmax_full_mha__prompt'

def setup_inputs():

    _w = (lambda a: (lambda t: t.to(torch.bfloat16) if t.is_floating_point() else t)(torch.as_tensor(a)))
    def _A_setup():
        B, S, D = 1, 2048, 576
        Df = D // 2 + 1
        return {
            "x": _w((lambda *s: torch.randn(*s, dtype=torch.bfloat16) * 0.05)(B, S, D)),
            "w_r": _w((lambda *s: torch.randn(*s, dtype=torch.bfloat16) * 1.6)(S, Df) * 0.02),
            "w_i": _w((lambda *s: torch.randn(*s, dtype=torch.bfloat16) * 1.6)(S, Df) * 0.02),
        }
    _A_in = _A_setup()

    def _B_setup():
        B, S, D = 1, 2048, 576
        return {
            "x": _w((lambda *s: torch.randn(*s, dtype=torch.bfloat16) * 0.05)(B, S, D)),
            "wqkv": _w((lambda *s: torch.randn(*s, dtype=torch.bfloat16) * 0.05)(D, 3 * D)),
            "wo": _w((lambda *s: torch.randn(*s, dtype=torch.bfloat16) * 0.05)(D, D)),
        }
    _B_in = _B_setup()

    return {
        "A_x": _A_in["x"],
        "A_w_r": _A_in["w_r"],
        "A_w_i": _A_in["w_i"],
        "B_x": _B_in["x"],
        "B_wqkv": _B_in["wqkv"],
        "B_wo": _B_in["wo"],
    }

def reference(A_x, A_w_r, A_w_i, B_x, B_wqkv, B_wo):
    def _A_ref(x, w_r, w_i):
        B, S, D = x.shape
        X = (lambda x, axes=(-2,-1): torch.fft.rfft2(x.float(), dim=axes))(x, axes=(-2, -1))
        W = w_r + 1j * w_i
        X = X * W
        y = (lambda x, s, axes=(-2,-1): torch.fft.irfft2(x, s=s, dim=axes).to(torch.bfloat16))(X, (S, D), axes=(-2, -1))
        return y

    def _B_ref(x, wqkv, wo):
        B, S, D = x.shape
        H = 32; Dh = D // H
        qkv = (lambda x, *d: x.permute(*d))((x @ wqkv).reshape(B, S, 3, H, Dh), 2, 0, 3, 1, 4)
        q, k, v = qkv[0], qkv[1], qkv[2]
        scale = Dh ** -0.5
        scores = torch.einsum("bhqd,bhkd->bhqk", q, k) * scale
        p = _softmax(scores, axis=-1)
        o = torch.einsum("bhqk,bhkd->bhqd", p, v)
        return (lambda x, *d: x.permute(*d))(o, 0, 2, 1, 3).reshape(B, S, D) @ wo

    x = A_x
    x = x + _A_ref(_layernorm(x, axis=-1), A_w_r, A_w_i)
    x = x + _B_ref(_layernorm(x, axis=-1), B_wqkv, B_wo)
    x = x + _A_ref(_layernorm(x, axis=-1), A_w_r, A_w_i)
    x = x + _B_ref(_layernorm(x, axis=-1), B_wqkv, B_wo)
    return x
```

```cpp
#include <hip/hip_runtime.h>
#include <hip/hip_bf16.h>

typedef __attribute__((ext_vector_type(8))) __bf16 bf8;
typedef __attribute__((ext_vector_type(2))) __bf16 bf2;
typedef __attribute__((ext_vector_type(8))) _Float16 h8;
typedef __attribute__((ext_vector_type(2))) _Float16 h2;
typedef __attribute__((ext_vector_type(4))) short s4;
typedef __attribute__((ext_vector_type(2))) float f2;
typedef __attribute__((ext_vector_type(4))) float f4;
typedef __attribute__((ext_vector_type(16))) float f16v;
typedef __attribute__((ext_vector_type(4))) unsigned u4;
typedef __attribute__((ext_vector_type(2))) unsigned u2;
typedef unsigned short u16;
#define LDS __attribute__((address_space(3)))
#define DEV __device__ __forceinline__

constexpr int S = 2048, D = 576;

struct CosT { u16 v[576]; };
#define COS_TABLE {{ \
0x3d71,0x3d71,0x3d71,0x3d71,0x3d71,0x3d71,0x3d71,0x3d71,0x3d70,0x3d70,0x3d70,0x3d70,0x3d6f,0x3d6f,0x3d6f,0x3d6e,0x3d6e,0x3d6d,0x3d6d,0x3d6c,0x3d6c,0x3d6b,0x3d6a,0x3d6a, \
0x3d69,0x3d68,0x3d68,0x3d67,0x3d66,0x3d65,0x3d65,0x3d64,0x3d63,0x3d62,0x3d61,0x3d60,0x3d5f,0x3d5e,0x3d5d,0x3d5c,0x3d5b,0x3d5a,0x3d58,0x3d57,0x3d56,0x3d55,0x3d54,0x3d52, \
0x3d51,0x3d50,0x3d4e,0x3d4d,0x3d4c,0x3d4a,0x3d49,0x3d47,0x3d46,0x3d44,0x3d43,0x3d41,0x3d3f,0x3d3e,0x3d3c,0x3d3b,0x3d39,0x3d37,0x3d35,0x3d34,0x3d32,0x3d30,0x3d2e,0x3d2d, \
0x3d2b,0x3d29,0x3d27,0x3d25,0x3d23,0x3d21,0x3d1f,0x3d1d,0x3d1b,0x3d19,0x3d17,0x3d15,0x3d13,0x3d11,0x3d0f,0x3d0d,0x3d0a,0x3d08,0x3d06,0x3d04,0x3d02,0x3cff,0x3cfa,0x3cf6, \
0x3cf1,0x3ced,0x3ce8,0x3ce4,0x3cdf,0x3cda,0x3cd6,0x3cd1,0x3ccc,0x3cc7,0x3cc2,0x3cbe,0x3cb9,0x3cb4,0x3caf,0x3caa,0x3ca5,0x3ca0,0x3c9b,0x3c96,0x3c91,0x3c8c,0x3c87,0x3c82, \
0x3c7a,0x3c70,0x3c65,0x3c5b,0x3c51,0x3c47,0x3c3c,0x3c32,0x3c28,0x3c1d,0x3c13,0x3c08,0x3bfc,0x3be7,0x3bd2,0x3bbd,0x3ba8,0x3b93,0x3b7d,0x3b53,0x3b28,0x3afd,0x3aa8,0x3a28, \
0x0000,0xba28,0xbaa8,0xbafd,0xbb28,0xbb53,0xbb7d,0xbb93,0xbba8,0xbbbd,0xbbd2,0xbbe7,0xbbfc,0xbc08,0xbc13,0xbc1d,0xbc28,0xbc32,0xbc3c,0xbc47,0xbc51,0xbc5b,0xbc65,0xbc70, \
0xbc7a,0xbc82,0xbc87,0xbc8c,0xbc91,0xbc96,0xbc9b,0xbca0,0xbca5,0xbcaa,0xbcaf,0xbcb4,0xbcb9,0xbcbe,0xbcc2,0xbcc7,0xbccc,0xbcd1,0xbcd6,0xbcda,0xbcdf,0xbce4,0xbce8,0xbced, \
0xbcf1,0xbcf6,0xbcfa,0xbcff,0xbd02,0xbd04,0xbd06,0xbd08,0xbd0a,0xbd0d,0xbd0f,0xbd11,0xbd13,0xbd15,0xbd17,0xbd19,0xbd1b,0xbd1d,0xbd1f,0xbd21,0xbd23,0xbd25,0xbd27,0xbd29, \
0xbd2b,0xbd2d,0xbd2e,0xbd30,0xbd32,0xbd34,0xbd35,0xbd37,0xbd39,0xbd3b,0xbd3c,0xbd3e,0xbd3f,0xbd41,0xbd43,0xbd44,0xbd46,0xbd47,0xbd49,0xbd4a,0xbd4c,0xbd4d,0xbd4e,0xbd50, \
0xbd51,0xbd52,0xbd54,0xbd55,0xbd56,0xbd57,0xbd58,0xbd5a,0xbd5b,0xbd5c,0xbd5d,0xbd5e,0xbd5f,0xbd60,0xbd61,0xbd62,0xbd63,0xbd64,0xbd65,0xbd65,0xbd66,0xbd67,0xbd68,0xbd68, \
0xbd69,0xbd6a,0xbd6a,0xbd6b,0xbd6c,0xbd6c,0xbd6d,0xbd6d,0xbd6e,0xbd6e,0xbd6f,0xbd6f,0xbd6f,0xbd70,0xbd70,0xbd70,0xbd70,0xbd71,0xbd71,0xbd71,0xbd71,0xbd71,0xbd71,0xbd71, \
0xbd71,0xbd71,0xbd71,0xbd71,0xbd71,0xbd71,0xbd71,0xbd71,0xbd70,0xbd70,0xbd70,0xbd70,0xbd6f,0xbd6f,0xbd6f,0xbd6e,0xbd6e,0xbd6d,0xbd6d,0xbd6c,0xbd6c,0xbd6b,0xbd6a,0xbd6a, \
0xbd69,0xbd68,0xbd68,0xbd67,0xbd66,0xbd65,0xbd65,0xbd64,0xbd63,0xbd62,0xbd61,0xbd60,0xbd5f,0xbd5e,0xbd5d,0xbd5c,0xbd5b,0xbd5a,0xbd58,0xbd57,0xbd56,0xbd55,0xbd54,0xbd52, \
0xbd51,0xbd50,0xbd4e,0xbd4d,0xbd4c,0xbd4a,0xbd49,0xbd47,0xbd46,0xbd44,0xbd43,0xbd41,0xbd3f,0xbd3e,0xbd3c,0xbd3b,0xbd39,0xbd37,0xbd35,0xbd34,0xbd32,0xbd30,0xbd2e,0xbd2d, \
0xbd2b,0xbd29,0xbd27,0xbd25,0xbd23,0xbd21,0xbd1f,0xbd1d,0xbd1b,0xbd19,0xbd17,0xbd15,0xbd13,0xbd11,0xbd0f,0xbd0d,0xbd0a,0xbd08,0xbd06,0xbd04,0xbd02,0xbcff,0xbcfa,0xbcf6, \
0xbcf1,0xbced,0xbce8,0xbce4,0xbcdf,0xbcda,0xbcd6,0xbcd1,0xbccc,0xbcc7,0xbcc2,0xbcbe,0xbcb9,0xbcb4,0xbcaf,0xbcaa,0xbca5,0xbca0,0xbc9b,0xbc96,0xbc91,0xbc8c,0xbc87,0xbc82, \
0xbc7a,0xbc70,0xbc65,0xbc5b,0xbc51,0xbc47,0xbc3c,0xbc32,0xbc28,0xbc1d,0xbc13,0xbc08,0xbbfc,0xbbe7,0xbbd2,0xbbbd,0xbba8,0xbb93,0xbb7d,0xbb53,0xbb28,0xbafd,0xbaa8,0xba28, \
0x0000,0x3a28,0x3aa8,0x3afd,0x3b28,0x3b53,0x3b7d,0x3b93,0x3ba8,0x3bbd,0x3bd2,0x3be7,0x3bfc,0x3c08,0x3c13,0x3c1d,0x3c28,0x3c32,0x3c3c,0x3c47,0x3c51,0x3c5b,0x3c65,0x3c70, \
0x3c7a,0x3c82,0x3c87,0x3c8c,0x3c91,0x3c96,0x3c9b,0x3ca0,0x3ca5,0x3caa,0x3caf,0x3cb4,0x3cb9,0x3cbe,0x3cc2,0x3cc7,0x3ccc,0x3cd1,0x3cd6,0x3cda,0x3cdf,0x3ce4,0x3ce8,0x3ced, \
0x3cf1,0x3cf6,0x3cfa,0x3cff,0x3d02,0x3d04,0x3d06,0x3d08,0x3d0a,0x3d0d,0x3d0f,0x3d11,0x3d13,0x3d15,0x3d17,0x3d19,0x3d1b,0x3d1d,0x3d1f,0x3d21,0x3d23,0x3d25,0x3d27,0x3d29, \
0x3d2b,0x3d2d,0x3d2e,0x3d30,0x3d32,0x3d34,0x3d35,0x3d37,0x3d39,0x3d3b,0x3d3c,0x3d3e,0x3d3f,0x3d41,0x3d43,0x3d44,0x3d46,0x3d47,0x3d49,0x3d4a,0x3d4c,0x3d4d,0x3d4e,0x3d50, \
0x3d51,0x3d52,0x3d54,0x3d55,0x3d56,0x3d57,0x3d58,0x3d5a,0x3d5b,0x3d5c,0x3d5d,0x3d5e,0x3d5f,0x3d60,0x3d61,0x3d62,0x3d63,0x3d64,0x3d65,0x3d65,0x3d66,0x3d67,0x3d68,0x3d68, \
0x3d69,0x3d6a,0x3d6a,0x3d6b,0x3d6c,0x3d6c,0x3d6d,0x3d6d,0x3d6e,0x3d6e,0x3d6f,0x3d6f,0x3d6f,0x3d70,0x3d70,0x3d70,0x3d70,0x3d71,0x3d71,0x3d71,0x3d71,0x3d71,0x3d71,0x3d71 \
}}
#define QKV_ASM \
  "buffer_load_dwordx4 v[32:35], %[xp], %[rs], %[so] offen sc1\n\ts_add_u32 %[so], %[so], 0x10000\n\tbuffer_load_dwordx4 v[36:39], %[xp], %[rs], %[so] offen sc1\n\t" \
  "ds_read_b128 v[104:107], %[wp] offset:19456\n\tds_read_b128 v[108:111], %[wt] offset:19456\n\tds_read_b128 v[112:115], %[wp] offset:21248\n\tds_read_b128 v[116:119], %[wt] offset:21248\n\t" \
  "s_add_u32 %[so], %[so], 0x10000\n\tbuffer_load_dwordx4 v[40:43], %[xp], %[rs], %[so] offen sc1\n\ts_add_u32 %[so], %[so], 0x10000\n\tbuffer_load_dwordx4 v[44:47], %[xp], %[rs], %[so] offen sc1\n\t" \
  "s_add_u32 %[so], %[so], 0x10000\n\tbuffer_load_dwordx4 v[48:51], %[xp], %[rs], %[so] offen sc1\n\ts_add_u32 %[so], %[so], 0x10000\n\tbuffer_load_dwordx4 v[52:55], %[xp], %[rs], %[so] offen sc1\n\t" \
  "s_add_u32 %[so], %[so], 0x10000\n\tbuffer_load_dwordx4 v[56:59], %[xp], %[rs], %[so] offen sc1\n\ts_add_u32 %[so], %[so], 0x10000\n\tbuffer_load_dwordx4 v[60:63], %[xp], %[rs], %[so] offen sc1\n\t" \
  "s_add_u32 %[so], %[so], 0x10000\n\tbuffer_load_dwordx4 v[64:67], %[xp], %[rs], %[so] offen sc1\n\ts_add_u32 %[so], %[so], 0x10000\n\tbuffer_load_dwordx4 v[68:71], %[xp], %[rs], %[so] offen sc1\n\t" \
  "s_add_u32 %[so], %[so], 0x10000\n\tbuffer_load_dwordx4 v[72:75], %[xp], %[rs], %[so] offen sc1\n\ts_add_u32 %[so], %[so], 0x10000\n\tbuffer_load_dwordx4 v[76:79], %[xp], %[rs], %[so] offen sc1\n\t" \
  "s_add_u32 %[so], %[so], 0x10000\n\tbuffer_load_dwordx4 v[80:83], %[xp], %[rs], %[so] offen sc1\n\ts_add_u32 %[so], %[so], 0x10000\n\tbuffer_load_dwordx4 v[84:87], %[xp], %[rs], %[so] offen sc1\n\t" \
  "s_add_u32 %[so], %[so], 0x10000\n\tbuffer_load_dwordx4 v[88:91], %[xp], %[rs], %[so] offen sc1\n\ts_add_u32 %[so], %[so], 0x10000\n\tbuffer_load_dwordx4 v[92:95], %[xp], %[rs], %[so] offen sc1\n\t" \
  "s_add_u32 %[so], %[so], 0x10000\n\tbuffer_load_dwordx4 v[96:99], %[xp], %[rs], %[so] offen sc1\n\ts_add_u32 %[so], %[so], 0x10000\n\tbuffer_load_dwordx4 v[100:103], %[xp], %[rs], %[so] offen sc1\n\t" \
  "s_lshl2_add_u32 %[t0], %[rg], %[ct]\n\tv_mov_b32 v120, %[t0]\n\ts_cmp_lt_u32 %[w], 8\n\ts_cbranch_scc1 2f\n\t1:\nds_read_b32 v121, v120 offset:64\n\tds_read_b128 v[0:3], %[a0] offset:0\n\t" \
  "ds_read_b128 v[4:7], %[a0] offset:1024\n\tds_read_b128 v[8:11], %[a0] offset:2048\n\tds_read_b128 v[12:15], %[a0] offset:3072\n\tds_read_b128 v[16:19], %[a1] offset:0\n\t" \
  "ds_read_b128 v[20:23], %[a1] offset:768\n\tds_read_b128 v[24:27], %[a1] offset:1536\n\tds_read_b128 v[28:31], %[a1] offset:2304\n\ts_waitcnt lgkmcnt(8)\n\tv_cmp_eq_u32 vcc, 0, v121\n\t" \
  "s_cbranch_vccz 4f\n\t3:\ns_sleep 2\n\tds_read_b32 v121, v120 offset:64\n\ts_sub_u32 %[n], %[n], 1\n\ts_cbranch_scc1 4f\n\ts_waitcnt lgkmcnt(0)\n\tv_cmp_eq_u32 vcc, 0, v121\n\ts_cbranch_vccnz 3b\n\t" \
  "s_branch 1b\n\t2:\nv_mov_b64 v[0:1], 0\n\tv_mov_b64 v[2:3], 0\n\tv_mov_b64 v[4:5], 0\n\tv_mov_b64 v[6:7], 0\n\tv_mov_b64 v[8:9], 0\n\tv_mov_b64 v[10:11], 0\n\tv_mov_b64 v[12:13], 0\n\t" \
  "v_mov_b64 v[14:15], 0\n\tv_mov_b64 v[16:17], 0\n\tv_mov_b64 v[18:19], 0\n\tv_mov_b64 v[20:21], 0\n\tv_mov_b64 v[22:23], 0\n\tv_mov_b64 v[24:25], 0\n\tv_mov_b64 v[26:27], 0\n\tv_mov_b64 v[28:29], 0\n\t" \
  "v_mov_b64 v[30:31], 0\n\t4:\ns_mov_b32 %[n], 0x40000\n\ts_waitcnt vmcnt(16)\n\tv_max3_u32 v120, v32, v33, v34\n\tv_max3_u32 v121, v36, v37, v38\n\tv_max3_u32 v120, v120, v35, v39\n\t" \
  "v_max_u32 v120, v120, v121\n\tv_cmp_eq_u32 vcc, -1, v120\n\t10:\ns_cbranch_vccnz 40f\n\t70:\nv_permlane16_swap_b32 v32, v34\n\tv_permlane16_swap_b32 v33, v35\n\tv_permlane16_swap_b32 v36, v38\n\t" \
  "v_permlane16_swap_b32 v37, v39\n\ts_waitcnt lgkmcnt(0)\n\tv_mfma_f32_32x32x16_bf16 v[0:15], v[104:107], v[32:35], v[0:15]\n\tds_read_b128 v[104:107], %[wp] offset:23040\n\ts_waitcnt vmcnt(14)\n\t" \
  "v_max3_u32 v120, v40, v41, v42\n\tv_max3_u32 v121, v44, v45, v46\n\tv_mfma_f32_32x32x16_bf16 v[16:31], v[32:35], v[108:111], v[16:31]\n\tds_read_b128 v[108:111], %[wt] offset:23040\n\t" \
  "ds_read_b128 v[32:35], %[wp] offset:24832\n\tv_max3_u32 v120, v120, v43, v47\n\tv_max_u32 v120, v120, v121\n\tv_cmp_eq_u32 vcc, -1, v120\n\ts_waitcnt lgkmcnt(3)\n\t" \
  "v_mfma_f32_32x32x16_bf16 v[0:15], v[112:115], v[36:39], v[0:15]\n\tds_read_b128 v[112:115], %[wt] offset:24832\n\t12:\ns_cbranch_vccnz 42f\n\t72:\nv_permlane16_swap_b32 v40, v42\n\t" \
  "v_permlane16_swap_b32 v41, v43\n\tv_mfma_f32_32x32x16_bf16 v[16:31], v[36:39], v[116:119], v[16:31]\n\tds_read_b128 v[116:119], %[wp] offset:26624\n\tds_read_b128 v[36:39], %[wt] offset:26624\n\t" \
  "v_permlane16_swap_b32 v44, v46\n\tv_permlane16_swap_b32 v45, v47\n\ts_waitcnt lgkmcnt(4)\n\tv_mfma_f32_32x32x16_bf16 v[0:15], v[104:107], v[40:43], v[0:15]\n\t" \
  "ds_read_b128 v[104:107], %[wp] offset:28416\n\ts_waitcnt vmcnt(12)\n\tv_max3_u32 v120, v48, v49, v50\n\tv_max3_u32 v121, v52, v53, v54\n\t" \
  "v_mfma_f32_32x32x16_bf16 v[16:31], v[40:43], v[108:111], v[16:31]\n\tds_read_b128 v[108:111], %[wt] offset:28416\n\tv_max3_u32 v120, v120, v51, v55\n\tv_max_u32 v120, v120, v121\n\t" \
  "v_cmp_eq_u32 vcc, -1, v120\n\ts_waitcnt lgkmcnt(4)\n\tv_mfma_f32_32x32x16_bf16 v[0:15], v[32:35], v[44:47], v[0:15]\n\tds_read_b128 v[40:43], %[wp] offset:30208\n\t14:\ns_cbranch_vccnz 44f\n\t74:\n" \
  "v_permlane16_swap_b32 v48, v50\n\tv_permlane16_swap_b32 v49, v51\n\tv_mfma_f32_32x32x16_bf16 v[16:31], v[44:47], v[112:115], v[16:31]\n\tds_read_b128 v[32:35], %[wt] offset:30208\n\t" \
  "v_permlane16_swap_b32 v52, v54\n\tv_permlane16_swap_b32 v53, v55\n\ts_waitcnt lgkmcnt(4)\n\tv_mfma_f32_32x32x16_bf16 v[0:15], v[116:119], v[48:51], v[0:15]\n\t" \
  "ds_read_b128 v[112:115], %[wp] offset:32000\n\ts_waitcnt vmcnt(10)\n\tv_max3_u32 v120, v56, v57, v58\n\tv_max3_u32 v121, v60, v61, v62\n\t" \
  "v_mfma_f32_32x32x16_bf16 v[16:31], v[48:51], v[36:39], v[16:31]\n\tds_read_b128 v[44:47], %[wt] offset:32000\n\tv_max3_u32 v120, v120, v59, v63\n\tv_max_u32 v120, v120, v121\n\t" \
  "v_cmp_eq_u32 vcc, -1, v120\n\ts_waitcnt lgkmcnt(4)\n\tv_mfma_f32_32x32x16_bf16 v[0:15], v[104:107], v[52:55], v[0:15]\n\tds_read_b128 v[116:119], %[wp] offset:33792\n\t16:\ns_cbranch_vccnz 46f\n\t" \
  "76:\nv_permlane16_swap_b32 v56, v58\n\tv_permlane16_swap_b32 v57, v59\n\tv_mfma_f32_32x32x16_bf16 v[16:31], v[52:55], v[108:111], v[16:31]\n\tds_read_b128 v[36:39], %[wt] offset:33792\n\t" \
  "v_permlane16_swap_b32 v60, v62\n\tv_permlane16_swap_b32 v61, v63\n\ts_waitcnt lgkmcnt(4)\n\tv_mfma_f32_32x32x16_bf16 v[0:15], v[40:43], v[56:59], v[0:15]\n\t" \
  "ds_read_b128 v[104:107], %[wp] offset:35584\n\ts_waitcnt vmcnt(8)\n\tv_max3_u32 v120, v64, v65, v66\n\tv_max3_u32 v121, v68, v69, v70\n\t" \
  "v_mfma_f32_32x32x16_bf16 v[16:31], v[56:59], v[32:35], v[16:31]\n\tds_read_b128 v[108:111], %[wt] offset:35584\n\tv_max3_u32 v120, v120, v67, v71\n\tv_max_u32 v120, v120, v121\n\t" \
  "v_cmp_eq_u32 vcc, -1, v120\n\ts_waitcnt lgkmcnt(4)\n\tv_mfma_f32_32x32x16_bf16 v[0:15], v[112:115], v[60:63], v[0:15]\n\tds_read_b128 v[40:43], %[wp] offset:37376\n\t18:\ns_cbranch_vccnz 48f\n\t78:\n" \
  "v_permlane16_swap_b32 v64, v66\n\tv_permlane16_swap_b32 v65, v67\n\tv_mfma_f32_32x32x16_bf16 v[16:31], v[60:63], v[44:47], v[16:31]\n\tds_read_b128 v[32:35], %[wt] offset:37376\n\t" \
  "v_permlane16_swap_b32 v68, v70\n\tv_permlane16_swap_b32 v69, v71\n\ts_waitcnt lgkmcnt(4)\n\tv_mfma_f32_32x32x16_bf16 v[0:15], v[116:119], v[64:67], v[0:15]\n\t" \
  "ds_read_b128 v[112:115], %[wp] offset:39168\n\ts_waitcnt vmcnt(6)\n\tv_max3_u32 v120, v72, v73, v74\n\tv_max3_u32 v121, v76, v77, v78\n\tv_mbcnt_lo_u32_b32 v51, -1, 0\n\t" \
  "v_mbcnt_hi_u32_b32 v51, -1, v51\n\tv_mfma_f32_32x32x16_bf16 v[16:31], v[64:67], v[36:39], v[16:31]\n\tds_read_b128 v[44:47], %[wt] offset:39168\n\tv_max3_u32 v120, v120, v75, v79\n\t" \
  "v_max_u32 v120, v120, v121\n\tv_cmp_eq_u32 vcc, -1, v120\n\ts_waitcnt lgkmcnt(4)\n\tv_mfma_f32_32x32x16_bf16 v[0:15], v[104:107], v[68:71], v[0:15]\n\tds_read_b128 v[116:119], %[wp] offset:40960\n\t" \
  "20:\ns_cbranch_vccnz 50f\n\t80:\nv_permlane16_swap_b32 v72, v74\n\tv_permlane16_swap_b32 v73, v75\n\tv_mfma_f32_32x32x16_bf16 v[16:31], v[68:71], v[108:111], v[16:31]\n\t" \
  "ds_read_b128 v[36:39], %[wt] offset:40960\n\tv_permlane16_swap_b32 v76, v78\n\tv_permlane16_swap_b32 v77, v79\n\ts_waitcnt lgkmcnt(4)\n\t" \
  "v_mfma_f32_32x32x16_bf16 v[0:15], v[40:43], v[72:75], v[0:15]\n\tds_read_b128 v[104:107], %[wp] offset:42752\n\ts_waitcnt vmcnt(4)\n\tv_max3_u32 v120, v80, v81, v82\n\t" \
  "v_max3_u32 v121, v84, v85, v86\n\tv_and_b32 v50, 31, v51\n\tv_lshrrev_b32 v51, 5, v51\n\tv_mfma_f32_32x32x16_bf16 v[16:31], v[72:75], v[32:35], v[16:31]\n\t" \
  "ds_read_b128 v[108:111], %[wt] offset:42752\n\tv_max3_u32 v120, v120, v83, v87\n\tv_max_u32 v120, v120, v121\n\tv_cmp_eq_u32 vcc, -1, v120\n\ts_waitcnt lgkmcnt(4)\n\t" \
  "v_mfma_f32_32x32x16_bf16 v[0:15], v[112:115], v[76:79], v[0:15]\n\tds_read_b128 v[40:43], %[wp] offset:44544\n\t22:\ns_cbranch_vccnz 52f\n\t82:\nv_permlane16_swap_b32 v80, v82\n\t" \
  "v_permlane16_swap_b32 v81, v83\n\tv_mfma_f32_32x32x16_bf16 v[16:31], v[76:79], v[44:47], v[16:31]\n\tds_read_b128 v[32:35], %[wt] offset:44544\n\tv_permlane16_swap_b32 v84, v86\n\t" \
  "v_permlane16_swap_b32 v85, v87\n\ts_waitcnt lgkmcnt(4)\n\tv_mfma_f32_32x32x16_bf16 v[0:15], v[116:119], v[80:83], v[0:15]\n\tds_read_b128 v[112:115], %[wp] offset:46336\n\ts_waitcnt vmcnt(2)\n\t" \
  "v_max3_u32 v120, v88, v89, v90\n\tv_max3_u32 v121, v92, v93, v94\n\ts_lshl_b32 %[t0], %[rg], 7\n\ts_add_u32 %[t0], %[t0], 160256\n\tv_lshl_add_u32 v54, v50, 2, %[t0]\n\t" \
  "v_lshl_add_u32 v55, v51, 4, %[t0]\n\tv_mfma_f32_32x32x16_bf16 v[16:31], v[80:83], v[36:39], v[16:31]\n\tds_read_b128 v[44:47], %[wt] offset:46336\n\tv_max3_u32 v120, v120, v91, v95\n\t" \
  "v_max_u32 v120, v120, v121\n\tv_cmp_eq_u32 vcc, -1, v120\n\ts_waitcnt lgkmcnt(4)\n\tv_mfma_f32_32x32x16_bf16 v[0:15], v[104:107], v[84:87], v[0:15]\n\tds_read_b128 v[116:119], %[wp] offset:48128\n\t" \
  "24:\ns_cbranch_vccnz 54f\n\t84:\nv_permlane16_swap_b32 v88, v90\n\tv_permlane16_swap_b32 v89, v91\n\tv_mfma_f32_32x32x16_bf16 v[16:31], v[84:87], v[108:111], v[16:31]\n\t" \
  "ds_read_b128 v[36:39], %[wt] offset:48128\n\tv_permlane16_swap_b32 v92, v94\n\tv_permlane16_swap_b32 v93, v95\n\ts_waitcnt lgkmcnt(4)\n\t" \
  "v_mfma_f32_32x32x16_bf16 v[0:15], v[40:43], v[88:91], v[0:15]\n\tds_read_b128 v[104:107], %[wp] offset:49920\n\ts_waitcnt vmcnt(0)\n\tv_max3_u32 v120, v96, v97, v98\n\t" \
  "v_max3_u32 v121, v100, v101, v102\n\tv_mov_b32 v59, %[ct]\n\tv_mul_u32_u24 v57, 352, v51\n\tv_mfma_f32_32x32x16_bf16 v[16:31], v[88:91], v[32:35], v[16:31]\n\t" \
  "ds_read_b128 v[108:111], %[wt] offset:49920\n\tds_read_b32 v49, v59 offset:128\n\tds_read_b32 v48, v54\n\tds_read_b128 v[80:83], v55 offset:0\n\tds_read_b128 v[84:87], v55 offset:32\n\t" \
  "ds_read_b128 v[64:67], v55 offset:64\n\tds_read_b128 v[68:71], v55 offset:96\n\tv_max3_u32 v120, v120, v99, v103\n\tv_max_u32 v120, v120, v121\n\tv_cmp_eq_u32 vcc, -1, v120\n\t" \
  "s_waitcnt lgkmcnt(10)\n\tv_mfma_f32_32x32x16_bf16 v[0:15], v[112:115], v[92:95], v[0:15]\n\t26:\ns_cbranch_vccnz 56f\n\t86:\nv_permlane16_swap_b32 v96, v98\n\tv_permlane16_swap_b32 v97, v99\n\t" \
  "v_mfma_f32_32x32x16_bf16 v[16:31], v[92:95], v[44:47], v[16:31]\n\tv_permlane16_swap_b32 v100, v102\n\tv_permlane16_swap_b32 v101, v103\n\ts_waitcnt lgkmcnt(8)\n\t" \
  "v_mfma_f32_32x32x16_bf16 v[0:15], v[116:119], v[96:99], v[0:15]\n\ts_lshl_b32 %[t0], %[rg], 9\n\ts_mul_i32 %[t1], %[rg], 1408\n\tv_lshl_add_u32 v52, v50, 4, %[t0]\n\t" \
  "v_lshl_add_u32 v57, v50, 4, v57\n\tv_mov_b32 v60, 0\n\tv_mov_b32 v61, -1\n\tv_mfma_f32_32x32x16_bf16 v[16:31], v[96:99], v[36:39], v[16:31]\n\tv_lshl_add_u32 v52, v51, 12, v52\n\t" \
  "v_add_u32 v57, %[t1], v57\n\ts_lshl_b32 %[t0], %[rg], 6\n\ts_add_u32 %[t0], %[t0], 145952\n\tv_max_u32 v58, 17, v50\n\tv_add_u32 v53, 146240, v52\n\tv_mov_b32 v56, v57\n\ts_waitcnt lgkmcnt(6)\n\t" \
  "v_mfma_f32_32x32x16_bf16 v[0:15], v[104:107], v[100:103], v[0:15]\n\ts_mov_b32 exec_lo, 0xc0000\n\ts_mov_b32 exec_hi, 0xc0000\n\tv_lshl_add_u32 v56, v50, 4, %[t0]\n\tv_mov_b32 v61, 0\n\t" \
  "v_lshl_add_u32 v56, v51, 5, v56\n\ts_mov_b32 exec_lo, 0x40000\n\ts_mov_b32 exec_hi, 0x40000\n\tv_mov_b32 v60, 0x3f803f80\n\tv_mov_b32 v61, 0x3f803f80\n\ts_mov_b64 exec, -1\n\t" \
  "v_mfma_f32_32x32x16_bf16 v[16:31], v[100:103], v[108:111], v[16:31]\n\ts_cmp_lt_u32 %[w], 8\n\ts_cbranch_scc1 5f\n\ts_cmp_lt_u32 %[w], 12\n\ts_cbranch_scc0 30f\n\ts_setprio 0\n\t30:\n" \
  "s_waitcnt lgkmcnt(0)\n\tv_cmp_ne_u32 vcc, 4, v49\n\t6:\ns_cbranch_vccnz 7f\n\t8:\nv_mul_f32 v8, v8, v48\n\tv_mul_f32 v9, v9, v48\n\tv_mul_f32 v10, v10, v48\n\tv_mul_f32 v11, v11, v48\n\t" \
  "v_mul_f32 v12, v12, v48\n\tv_mul_f32 v13, v13, v48\n\tv_mul_f32 v14, v14, v48\n\tv_mul_f32 v15, v15, v48\n\tv_cvt_pk_bf16_f32 v96, v8, v9\n\tv_cvt_pk_bf16_f32 v97, v10, v11\n\t" \
  "v_cvt_pk_bf16_f32 v98, v12, v13\n\tv_cvt_pk_bf16_f32 v99, v14, v15\n\tds_write_b128 v52, v[96:99]\n\tbuffer_store_dwordx4 v[96:99], v52, %[rs], %[kv] offen sc1\n\tv_mul_f32 v0, v0, v48\n\t" \
  "v_mul_f32 v1, v1, v48\n\tv_mul_f32 v2, v2, v48\n\tv_mul_f32 v3, v3, v48\n\tv_mul_f32 v4, v4, v48\n\tv_mul_f32 v5, v5, v48\n\tv_mul_f32 v6, v6, v48\n\tv_mul_f32 v7, v7, v48\n\t" \
  "v_cvt_pk_bf16_f32 v100, v0, v1\n\tv_cvt_pk_bf16_f32 v101, v2, v3\n\tv_cvt_pk_bf16_f32 v102, v4, v5\n\tv_cvt_pk_bf16_f32 v103, v6, v7\n\tds_write_b128 v53, v[100:103]\n\ts_mov_b32 exec_lo, 0x3fffff\n\t" \
  "s_mov_b32 exec_hi, 0x3fffff\n\tv_mul_f32 v16, v16, v80\n\tv_mul_f32 v17, v17, v81\n\tv_mul_f32 v18, v18, v82\n\tv_mul_f32 v19, v19, v83\n\tv_mul_f32 v20, v20, v84\n\tv_mul_f32 v21, v21, v85\n\t" \
  "v_mul_f32 v22, v22, v86\n\tv_mul_f32 v23, v23, v87\n\tv_cvt_pk_bf16_f32 v88, v16, v17\n\tv_cvt_pk_bf16_f32 v89, v18, v19\n\tv_cvt_pk_bf16_f32 v90, v20, v21\n\tv_cvt_pk_bf16_f32 v91, v22, v23\n\t" \
  "ds_write_b128 v56, v[88:91] offset:8192\n\tv_mul_f32 v24, v24, v64\n\tv_mul_f32 v25, v25, v65\n\tv_mul_f32 v26, v26, v66\n\tv_mul_f32 v27, v27, v67\n\tv_mul_f32 v28, v28, v68\n\t" \
  "v_mul_f32 v29, v29, v69\n\tv_mul_f32 v30, v30, v70\n\tv_mul_f32 v31, v31, v71\n\tv_cvt_pk_bf16_f32 v92, v24, v25\n\tv_cvt_pk_bf16_f32 v93, v26, v27\n\tv_cvt_pk_bf16_f32 v94, v28, v29\n\t" \
  "v_cvt_pk_bf16_f32 v95, v30, v31\n\tds_write_b128 v56, v[92:95] offset:8896\n\ts_add_u32 %[t0], %[kv], 8192\n\tv_med3_u32 v72, v88, v60, v61\n\tv_med3_u32 v73, v89, v60, v61\n\t" \
  "v_med3_u32 v74, v90, v60, v61\n\tv_med3_u32 v75, v91, v60, v61\n\tbuffer_store_dwordx4 v[72:75], v57, %[rs], %[t0] offen sc1\n\tv_med3_u32 v76, v92, v60, v61\n\tv_med3_u32 v77, v93, v60, v61\n\t" \
  "v_med3_u32 v78, v94, v60, v61\n\tv_med3_u32 v79, v95, v60, v61\n\tbuffer_store_dwordx4 v[76:79], v57, %[rs], %[t0] offen offset:704 sc1\n\ts_mov_b64 exec, -1\n\tv_mul_f32 v64, v0, v0\n\t" \
  "v_mul_f32 v65, v8, v8\n\tv_fmac_f32 v64, v1, v1\n\tv_fmac_f32 v65, v9, v9\n\tv_fmac_f32 v64, v2, v2\n\tv_fmac_f32 v65, v10, v10\n\tv_fmac_f32 v64, v3, v3\n\tv_fmac_f32 v65, v11, v11\n\t" \
  "v_fmac_f32 v64, v4, v4\n\tv_fmac_f32 v65, v12, v12\n\tv_fmac_f32 v64, v5, v5\n\tv_fmac_f32 v65, v13, v13\n\tv_fmac_f32 v64, v6, v6\n\tv_fmac_f32 v65, v14, v14\n\tv_fmac_f32 v64, v7, v7\n\t" \
  "v_fmac_f32 v65, v15, v15\n\tv_max3_f32 v66, |v16|, |v17|, |v18|\n\tv_max3_f32 v67, |v19|, |v20|, |v21|\n\tv_max3_f32 v68, |v22|, |v23|, |v24|\n\tv_permlane32_swap_b32 v64, v65\n\t" \
  "v_max3_f32 v69, |v25|, |v26|, |v27|\n\tv_max3_f32 v70, |v28|, |v29|, |v30|\n\tv_max3_f32 v66, |v66|, |v67|, |v68|\n\tv_max3_f32 v69, |v69|, |v70|, |v31|\n\tv_add_f32 v64, v64, v65\n\t" \
  "v_max_f32 v66, v66, v69\n\tv_lshl_add_u32 v51, v51, 2, %[ct]\n\tv_max_u32_dpp v64, v64, v64 row_ror:8 row_mask:0xf bank_mask:0xf bound_ctrl:1\n\t" \
  "v_max_u32_dpp v65, v66, v66 row_ror:8 row_mask:0xf bank_mask:0xf bound_ctrl:1\n\tv_lshl_add_u32 v58, v58, 2, %[ct]\n\tv_max_u32_dpp v64, v64, v64 row_ror:4 row_mask:0xf bank_mask:0xf bound_ctrl:1\n\t" \
  "v_max_u32_dpp v65, v65, v65 row_ror:4 row_mask:0xf bank_mask:0xf bound_ctrl:1\n\ts_nop 0\n\tv_max_u32_dpp v64, v64, v64 row_ror:2 row_mask:0xf bank_mask:0xf bound_ctrl:1\n\t" \
  "v_max_u32_dpp v65, v65, v65 row_ror:2 row_mask:0xf bank_mask:0xf bound_ctrl:1\n\ts_nop 0\n\tv_max_u32_dpp v64, v64, v64 row_ror:1 row_mask:0xf bank_mask:0xf bound_ctrl:1\n\t" \
  "v_max_u32_dpp v65, v65, v65 row_ror:1 row_mask:0xf bank_mask:0xf bound_ctrl:1\n\ts_mov_b32 exec_lo, 0x3f0000\n\ts_mov_b32 exec_hi, 0x3f0000\n\tds_max_u32 v58, v66 offset:76\n\t" \
  "s_mov_b32 exec_lo, 0x10001\n\ts_mov_b32 exec_hi, 0x10001\n\tds_max_u32 v51, v64\n\ts_mov_b32 exec_lo, 0x1\n\ts_mov_b32 exec_hi, 0x1\n\tds_max_u32 v58, v65 offset:76\n\ts_mov_b64 exec, -1\n\t" \
  "s_branch 99f\n\t5:\ns_lshl2_add_u32 %[t0], %[rg], %[ct]\n\tv_mov_b32 v120, %[t0]\n\tv_mov_b32 v121, 1\n\tds_write_b128 %[a0], v[0:3] offset:0\n\tds_write_b128 %[a0], v[4:7] offset:1024\n\t" \
  "ds_write_b128 %[a0], v[8:11] offset:2048\n\tds_write_b128 %[a0], v[12:15] offset:3072\n\ts_mov_b32 exec_lo, 0x3fffff\n\ts_mov_b32 exec_hi, 0x3fffff\n\ts_nop 0\n\t" \
  "ds_write_b128 %[a1], v[16:19] offset:0\n\tds_write_b128 %[a1], v[20:23] offset:768\n\tds_write_b128 %[a1], v[24:27] offset:1536\n\tds_write_b128 %[a1], v[28:31] offset:2304\n\t" \
  "ds_write_b32 v120, v121 offset:64\n\ts_mov_b64 exec, -1\n\ts_waitcnt lgkmcnt(9)\n\ts_branch 99f\n\t40:\ns_sub_u32 %[t0], %[so], 0x110000\n\ts_add_u32 %[t1], %[t0], 0x10000\n\t" \
  "buffer_load_dwordx4 v[32:35], %[xp], %[rs], %[t0] offen sc1\n\tbuffer_load_dwordx4 v[36:39], %[xp], %[rs], %[t1] offen sc1\n\ts_waitcnt vmcnt(0)\n\tv_max3_u32 v120, v32, v33, v34\n\t" \
  "v_max3_u32 v121, v36, v37, v38\n\tv_max3_u32 v120, v120, v35, v39\n\tv_max_u32 v120, v120, v121\n\tv_cmp_eq_u32 vcc, -1, v120\n\ts_sub_u32 %[n], %[n], 1\n\ts_cbranch_scc0 10b\n\ts_mov_b32 %[n], 0\n\t" \
  "s_branch 70b\n\t42:\ns_sub_u32 %[t0], %[so], 0xf0000\n\ts_add_u32 %[t1], %[t0], 0x10000\n\tbuffer_load_dwordx4 v[40:43], %[xp], %[rs], %[t0] offen sc1\n\t" \
  "buffer_load_dwordx4 v[44:47], %[xp], %[rs], %[t1] offen sc1\n\ts_waitcnt vmcnt(0)\n\tv_max3_u32 v120, v40, v41, v42\n\tv_max3_u32 v121, v44, v45, v46\n\tv_max3_u32 v120, v120, v43, v47\n\t" \
  "v_max_u32 v120, v120, v121\n\tv_cmp_eq_u32 vcc, -1, v120\n\ts_sub_u32 %[n], %[n], 1\n\ts_cbranch_scc0 12b\n\ts_mov_b32 %[n], 0\n\ts_branch 72b\n\t44:\ns_sub_u32 %[t0], %[so], 0xd0000\n\t" \
  "s_add_u32 %[t1], %[t0], 0x10000\n\tbuffer_load_dwordx4 v[48:51], %[xp], %[rs], %[t0] offen sc1\n\tbuffer_load_dwordx4 v[52:55], %[xp], %[rs], %[t1] offen sc1\n\ts_waitcnt vmcnt(0)\n\t" \
  "v_max3_u32 v120, v48, v49, v50\n\tv_max3_u32 v121, v52, v53, v54\n\tv_max3_u32 v120, v120, v51, v55\n\tv_max_u32 v120, v120, v121\n\tv_cmp_eq_u32 vcc, -1, v120\n\ts_sub_u32 %[n], %[n], 1\n\t" \
  "s_cbranch_scc0 14b\n\ts_mov_b32 %[n], 0\n\ts_branch 74b\n\t46:\ns_sub_u32 %[t0], %[so], 0xb0000\n\ts_add_u32 %[t1], %[t0], 0x10000\n\tbuffer_load_dwordx4 v[56:59], %[xp], %[rs], %[t0] offen sc1\n\t" \
  "buffer_load_dwordx4 v[60:63], %[xp], %[rs], %[t1] offen sc1\n\ts_waitcnt vmcnt(0)\n\tv_max3_u32 v120, v56, v57, v58\n\tv_max3_u32 v121, v60, v61, v62\n\tv_max3_u32 v120, v120, v59, v63\n\t" \
  "v_max_u32 v120, v120, v121\n\tv_cmp_eq_u32 vcc, -1, v120\n\ts_sub_u32 %[n], %[n], 1\n\ts_cbranch_scc0 16b\n\ts_mov_b32 %[n], 0\n\ts_branch 76b\n\t48:\ns_sub_u32 %[t0], %[so], 0x90000\n\t" \
  "s_add_u32 %[t1], %[t0], 0x10000\n\tbuffer_load_dwordx4 v[64:67], %[xp], %[rs], %[t0] offen sc1\n\tbuffer_load_dwordx4 v[68:71], %[xp], %[rs], %[t1] offen sc1\n\ts_waitcnt vmcnt(0)\n\t" \
  "v_max3_u32 v120, v64, v65, v66\n\tv_max3_u32 v121, v68, v69, v70\n\tv_max3_u32 v120, v120, v67, v71\n\tv_max_u32 v120, v120, v121\n\tv_cmp_eq_u32 vcc, -1, v120\n\ts_sub_u32 %[n], %[n], 1\n\t" \
  "s_cbranch_scc0 18b\n\ts_mov_b32 %[n], 0\n\ts_branch 78b\n\t50:\ns_sub_u32 %[t0], %[so], 0x70000\n\ts_add_u32 %[t1], %[t0], 0x10000\n\tbuffer_load_dwordx4 v[72:75], %[xp], %[rs], %[t0] offen sc1\n\t" \
  "buffer_load_dwordx4 v[76:79], %[xp], %[rs], %[t1] offen sc1\n\ts_waitcnt vmcnt(0)\n\tv_max3_u32 v120, v72, v73, v74\n\tv_max3_u32 v121, v76, v77, v78\n\tv_max3_u32 v120, v120, v75, v79\n\t" \
  "v_max_u32 v120, v120, v121\n\tv_cmp_eq_u32 vcc, -1, v120\n\ts_sub_u32 %[n], %[n], 1\n\ts_cbranch_scc0 20b\n\ts_mov_b32 %[n], 0\n\ts_branch 80b\n\t52:\ns_sub_u32 %[t0], %[so], 0x50000\n\t" \
  "s_add_u32 %[t1], %[t0], 0x10000\n\tbuffer_load_dwordx4 v[80:83], %[xp], %[rs], %[t0] offen sc1\n\tbuffer_load_dwordx4 v[84:87], %[xp], %[rs], %[t1] offen sc1\n\ts_waitcnt vmcnt(0)\n\t" \
  "v_max3_u32 v120, v80, v81, v82\n\tv_max3_u32 v121, v84, v85, v86\n\tv_max3_u32 v120, v120, v83, v87\n\tv_max_u32 v120, v120, v121\n\tv_cmp_eq_u32 vcc, -1, v120\n\ts_sub_u32 %[n], %[n], 1\n\t" \
  "s_cbranch_scc0 22b\n\ts_mov_b32 %[n], 0\n\ts_branch 82b\n\t54:\ns_sub_u32 %[t0], %[so], 0x30000\n\ts_add_u32 %[t1], %[t0], 0x10000\n\tbuffer_load_dwordx4 v[88:91], %[xp], %[rs], %[t0] offen sc1\n\t" \
  "buffer_load_dwordx4 v[92:95], %[xp], %[rs], %[t1] offen sc1\n\ts_waitcnt vmcnt(0)\n\tv_max3_u32 v120, v88, v89, v90\n\tv_max3_u32 v121, v92, v93, v94\n\tv_max3_u32 v120, v120, v91, v95\n\t" \
  "v_max_u32 v120, v120, v121\n\tv_cmp_eq_u32 vcc, -1, v120\n\ts_sub_u32 %[n], %[n], 1\n\ts_cbranch_scc0 24b\n\ts_mov_b32 %[n], 0\n\ts_branch 84b\n\t56:\ns_sub_u32 %[t0], %[so], 0x10000\n\t" \
  "s_add_u32 %[t1], %[t0], 0x10000\n\tbuffer_load_dwordx4 v[96:99], %[xp], %[rs], %[t0] offen sc1\n\tbuffer_load_dwordx4 v[100:103], %[xp], %[rs], %[t1] offen sc1\n\ts_waitcnt vmcnt(0)\n\t" \
  "v_max3_u32 v120, v96, v97, v98\n\tv_max3_u32 v121, v100, v101, v102\n\tv_max3_u32 v120, v120, v99, v103\n\tv_max_u32 v120, v120, v121\n\tv_cmp_eq_u32 vcc, -1, v120\n\ts_sub_u32 %[n], %[n], 1\n\t" \
  "s_cbranch_scc0 26b\n\ts_mov_b32 %[n], 0\n\ts_branch 86b\n\t7:\ns_sleep 4\n\tds_read_b32 v49, v59 offset:128\n\tds_read_b32 v48, v54\n\tds_read_b128 v[80:83], v55 offset:0\n\t" \
  "ds_read_b128 v[84:87], v55 offset:32\n\tds_read_b128 v[64:67], v55 offset:64\n\tds_read_b128 v[68:71], v55 offset:96\n\ts_waitcnt lgkmcnt(0)\n\tv_cmp_ne_u32 vcc, 4, v49\n\ts_sub_u32 %[n], %[n], 1\n\t" \
  "s_cbranch_scc0 6b\n\ts_branch 8b\n\t99:\n"
#define QKV_BW 16
#define QKV_CLOB "v0", "v1", "v2", "v3", "v4", "v5", "v6", "v7", "v8", "v9", "v10", "v11", "v12", "v13", "v14", "v15", "v16", "v17", "v18", "v19", "v20", "v21", "v22", "v23", "v24", "v25", "v26", "v27", "v28", "v29", "v30", "v31", "v32", "v33", "v34", "v35", "v36", "v37", "v38", "v39", "v40", "v41", "v42", "v43", "v44", "v45", "v46", "v47", "v48", "v49", "v50", "v51", "v52", "v53", "v54", "v55", "v56", "v57", "v58", "v59", "v60", "v61", "v62", "v63", "v64", "v65", "v66", "v67", "v68", "v69", "v70", "v71", "v72", "v73", "v74", "v75", "v76", "v77", "v78", "v79", "v80", "v81", "v82", "v83", "v84", "v85", "v86", "v87", "v88", "v89", "v90", "v91", "v92", "v93", "v94", "v95", "v96", "v97", "v98", "v99", "v100", "v101", "v102", "v103", "v104", "v105", "v106", "v107", "v108", "v109", "v110", "v111", "v112", "v113", "v114", "v115", "v116", "v117", "v118", "v119", "v120", "v121"
#define GAT_ASM \
  "s_add_u32 %[t], %[so], 4096\n\t1:\nbuffer_load_dwordx4 v[92:95], %[vo], %[rs], %[so] offen offset:0 sc1\n\tbuffer_load_dwordx4 v[96:99], %[vo], %[rs], %[so] offen offset:1024 sc1\n\t" \
  "buffer_load_dwordx4 v[100:103], %[vo], %[rs], %[so] offen offset:2048 sc1\n\tbuffer_load_dwordx4 v[104:107], %[vo], %[rs], %[so] offen offset:3072 sc1\n\t" \
  "buffer_load_dwordx4 v[108:111], %[vo], %[rs], %[t] offen offset:0 sc1\n\tbuffer_load_dwordx4 v[112:115], %[vo], %[rs], %[t] offen offset:1024 sc1\n\t" \
  "buffer_load_dwordx4 v[116:119], %[vo], %[rs], %[t] offen offset:2048 sc1\n\tbuffer_load_dwordx4 v[120:123], %[vo], %[rs], %[t] offen offset:3072 sc1\n\t" \
  "buffer_load_dwordx4 v[124:127], %[vo], %[rs], %[st] offen sc1\n\tds_write_b32 %[lf], %[lf] offset:16\n\ts_branch 3f\n\t2:\ns_sleep 2\n\ts_sub_u32 %[n], %[n], 1\n\ts_cbranch_scc1 9f\n\t3:\n" \
  "ds_read_b32 v91, %[ld]\n\ts_waitcnt lgkmcnt(0)\n\tv_cmp_eq_u32 vcc, 0, v91\n\ts_cbranch_vccnz 2b\n\ts_waitcnt vmcnt(8)\n\tds_write_b128 %[la], v[92:95] offset:0\n\tv_max3_u32 v91, v91, v92, v93\n\t" \
  "v_max3_u32 v91, v91, v94, v95\n\ts_waitcnt vmcnt(7)\n\tds_write_b128 %[la], v[96:99] offset:1024\n\tv_max3_u32 v91, v91, v96, v97\n\tv_max3_u32 v91, v91, v98, v99\n\ts_waitcnt vmcnt(6)\n\t" \
  "ds_write_b128 %[la], v[100:103] offset:2048\n\tv_max3_u32 v91, v91, v100, v101\n\tv_max3_u32 v91, v91, v102, v103\n\ts_waitcnt vmcnt(5)\n\tds_write_b128 %[la], v[104:107] offset:3072\n\t" \
  "v_max3_u32 v91, v91, v104, v105\n\tv_max3_u32 v91, v91, v106, v107\n\ts_waitcnt vmcnt(4)\n\tds_write_b128 %[la], v[108:111] offset:4096\n\tv_max3_u32 v91, v91, v108, v109\n\t" \
  "v_max3_u32 v91, v91, v110, v111\n\ts_waitcnt vmcnt(3)\n\tds_write_b128 %[la], v[112:115] offset:5120\n\tv_max3_u32 v91, v91, v112, v113\n\tv_max3_u32 v91, v91, v114, v115\n\ts_waitcnt vmcnt(2)\n\t" \
  "ds_write_b128 %[la], v[116:119] offset:6144\n\tv_max3_u32 v91, v91, v116, v117\n\tv_max3_u32 v91, v91, v118, v119\n\ts_waitcnt vmcnt(1)\n\tds_write_b128 %[la], v[120:123] offset:7168\n\t" \
  "v_max3_u32 v91, v91, v120, v121\n\tv_max3_u32 v91, v91, v122, v123\n\ts_waitcnt vmcnt(0)\n\tds_write2_b32 %[lt], v124, v125 offset1:4\n\tds_write2_b32 %[lt], v126, v127 offset0:8 offset1:12\n\t" \
  "v_max3_u32 v91, v91, v124, v125\n\tv_max3_u32 v91, v91, v126, v127\n\tv_cmp_eq_u32 vcc, -1, v91\n\ts_cbranch_vccz 9f\n\ts_waitcnt lgkmcnt(0)\n\ts_sub_u32 %[n], %[n], 1\n\ts_cbranch_scc0 1b\n\t9:\n" \
  "ds_write_b32 %[lf], %[lf]\n\ts_waitcnt vmcnt(0) lgkmcnt(0)\n\t"
#define STG_ASM \
  "buffer_load_dwordx4 v[92:95], %[vo], %[rs], %[so] offen sc1\n\ts_add_u32 %[so], %[so], 0x10000\n\tbuffer_load_dwordx4 v[96:99], %[vo], %[rs], %[so] offen sc1\n\ts_add_u32 %[so], %[so], 0x10000\n\t" \
  "buffer_load_dwordx4 v[100:103], %[vo], %[rs], %[so] offen sc1\n\ts_add_u32 %[so], %[so], 0x10000\n\tbuffer_load_dwordx4 v[104:107], %[vo], %[rs], %[so] offen sc1\n\ts_add_u32 %[so], %[so], 0x10000\n\t" \
  "buffer_load_dwordx4 v[108:111], %[vo], %[rs], %[so] offen sc1\n\ts_add_u32 %[so], %[so], 0x10000\n\tbuffer_load_dwordx4 v[112:115], %[vo], %[rs], %[so] offen sc1\n\ts_add_u32 %[so], %[so], 0x10000\n\t" \
  "buffer_load_dwordx4 v[116:119], %[vo], %[rs], %[so] offen sc1\n\ts_add_u32 %[so], %[so], 0x10000\n\tbuffer_load_dwordx4 v[120:123], %[vo], %[rs], %[so] offen sc1\n\ts_add_u32 %[so], %[so], 0x10000\n\t" \
  "buffer_load_dwordx4 v[124:127], %[vo], %[rs], %[so] offen sc1\n\ts_waitcnt vmcnt(8)\n\tv_permlane16_swap_b32 v92, v94\n\tv_permlane16_swap_b32 v93, v95\n\ts_waitcnt vmcnt(7)\n\t" \
  "v_permlane16_swap_b32 v96, v98\n\tv_permlane16_swap_b32 v97, v99\n\tds_write_b128 %[la], v[92:95] offset:0\n\ts_waitcnt vmcnt(6)\n\tv_permlane16_swap_b32 v100, v102\n\t" \
  "v_permlane16_swap_b32 v101, v103\n\tds_write_b128 %[la], v[96:99] offset:1024\n\ts_waitcnt vmcnt(5)\n\tv_permlane16_swap_b32 v104, v106\n\tv_permlane16_swap_b32 v105, v107\n\t" \
  "ds_write_b128 %[la], v[100:103] offset:2048\n\ts_waitcnt vmcnt(4)\n\tv_permlane16_swap_b32 v108, v110\n\tv_permlane16_swap_b32 v109, v111\n\tds_write_b128 %[la], v[104:107] offset:3072\n\t" \
  "s_waitcnt vmcnt(3)\n\tv_permlane16_swap_b32 v112, v114\n\tv_permlane16_swap_b32 v113, v115\n\tds_write_b128 %[la], v[108:111] offset:4096\n\ts_waitcnt vmcnt(2)\n\tv_permlane16_swap_b32 v116, v118\n\t" \
  "v_permlane16_swap_b32 v117, v119\n\tds_write_b128 %[la], v[112:115] offset:5120\n\ts_waitcnt vmcnt(1)\n\tv_permlane16_swap_b32 v120, v122\n\tv_permlane16_swap_b32 v121, v123\n\t" \
  "ds_write_b128 %[la], v[116:119] offset:6144\n\ts_waitcnt vmcnt(0)\n\tv_permlane16_swap_b32 v124, v126\n\tv_permlane16_swap_b32 v125, v127\n\tds_write_b128 %[la], v[120:123] offset:7168\n\t" \
  "ds_write_b128 %[la], v[124:127] offset:8192\n\ts_waitcnt lgkmcnt(0)\n\t"
#define GAT_CLOB "v91", "v92", "v93", "v94", "v95", "v96", "v97", "v98", "v99", "v100", "v101", "v102", "v103", "v104", "v105", "v106", "v107", "v108", "v109", "v110", "v111", "v112", "v113", "v114", "v115", "v116", "v117", "v118", "v119", "v120", "v121", "v122", "v123", "v124", "v125", "v126", "v127"
__device__ __attribute__((aligned(64))) const CosT CT_DEV = COS_TABLE;
#define CMAP(P) ((P) <= 72 ? 2 * (P) : (P) < 144 ? 432 - 2 * (P) : (P) < 216 ? 2 * (P) - 287 : 719 - 2 * (P))
#define RV(T, d, j) ((j) >= 2 ? T.v[((d) * CMAP((j) >> 1) + ((j) & 1) * 144) % 576] : (u16)(0x3d2b | (((d) & ~(j) & 1) << 15)))

struct FftT { unsigned v[34 * 256]; };
constexpr FftT fft_tables() {
  double c[2048] = {};
  for (int m = 0; m <= 512; m++) {
    const double x = 6.283185307179586 * m / 2048, y = x * x;
    double t = 1, q = 1;
    for (int i = 1; i < 10; i++) { t *= -y / ((2 * i - 1) * 2 * i); q += t; }
    c[m] = q; c[1024 - m] = -q; c[1024 + m] = -q; c[(2048 - m) & 2047] = q;
  }
  FftT T = {};
  auto put = [&](int o, int m, double a, double b) {
    T.v[o] = __builtin_bit_cast(u16, (_Float16)(a * c[m & 2047])) | (unsigned)__builtin_bit_cast(u16, (_Float16)(b * c[(m + 1536) & 2047])) << 16;
  };
  for (int e = 0; e < 256; e++) {
    const int lr = (e >> 2) & 15, g = e >> 6, i = e & 3;
    for (int r = 0; r < 8; r++) {
      put(r * 1024 + e, 16 * (4 * g + (i ^ (r >> 1))) * (8 * lr + r), 6, 6);
      put(r * 1024 + 256 + e, (4 * g + i) * (8 * lr + r), 1, -1);
      put(r * 1024 + 512 + e, lr * (8 * (4 * g + i) + r), 1, -1);
      put(r * 1024 + 768 + e, 16 * lr * (8 * (4 * g + i) + r), 0.25, 0.25);
    }
    put(8192 + e, 128 * lr * (4 * g + i), 0.25, 0.25);
    if (i < 2) put(8448 + (e >> 2) * 2 + i, 256 * (2 * g + i) * (lr >> 1) + (lr & 1) * 512, 0.25, 0.25);
  }
  return T;
}
__device__ __attribute__((aligned(128))) const FftT FT_DEV = fft_tables();

constexpr size_t ACT = (size_t)S * D * 2;
constexpr size_t WSQ = (size_t)D * D * 2;
constexpr int CHB = 19456;
constexpr size_t XCH = ACT + 256 * CHB;
constexpr size_t O_XT0 = 0, O_XT1 = ACT, O_SS = 2 * ACT, O_FLG = O_SS + 32768, O_M2 = O_FLG + 8192, O_MW = O_M2 + 8192, O_M1 = O_MW + 16384, O_M3 = O_M1 + 40960, O_EP = O_M3 + 65536, O_SQ = O_EP + 4096, O_FK = O_SQ + 65536, O_H = O_FK + 65536,
                 O_XT3 = 3 * ACT, O_WT = 4 * ACT, O_WQ2 = 6 * ACT, O_WO2 = 7 * ACT, O_RT = O_WO2 + WSQ, O_WOT = O_RT + WSQ,
                 O_XT2 = 8 * ACT, O_SS1 = O_XT2 - 32768, O_X = 9 * ACT, WS_END = O_X + 2 * XCH;

#define STAMP_END(k)
#define STAMP(i)
#define ASTAMP(i)
#define STAMP_DECL
#define CYC0()
#define CYC1(i)
#define TPAR
#define TARG
#define STAMP_OUT(k, wv)
DEV int lane_id() { int l; asm volatile("v_mbcnt_lo_u32_b32 %0, -1, 0\n\tv_mbcnt_hi_u32_b32 %0, -1, %0" : "=v"(l)); __builtin_assume(l >= 0 && l < 64); return l; }
DEV unsigned pkbf(float a, float b) { f2 v = {a, b}; return __builtin_bit_cast(unsigned, __builtin_convertvector(v, bf2)); }
DEV unsigned pkh(float a, float b) { f2 v = {a, b}; return __builtin_bit_cast(unsigned, __builtin_convertvector(v, h2)); }
DEV float bflo(unsigned u) { return __uint_as_float(u << 16); }
DEV float bfhi(unsigned u) { return __uint_as_float(u & 0xffff0000u); }
DEV float rbf(float a) { return bflo(pkbf(a, a)); }
DEV float ln_rstd(float ss) {
  float v = rbf(ss * (1.0f / 576.0f));
  v = rbf(v + 1e-5f);
  v = rbf(__builtin_amdgcn_sqrtf(v));
  return __builtin_amdgcn_rcpf(v);
}
DEV f4 mfma16(u4 a, u4 b, f4 c) { return __builtin_amdgcn_mfma_f32_16x16x32_bf16(__builtin_bit_cast(bf8, a), __builtin_bit_cast(bf8, b), c, 0, 0, 0); }
DEV f4 mfma16h(u4 a, u4 b, f4 c) { return __builtin_amdgcn_mfma_f32_16x16x32_f16(__builtin_bit_cast(h8, a), __builtin_bit_cast(h8, b), c, 0, 0, 0); }
DEV f16v mfma32(u4 a, u4 b, f16v c) { return __builtin_amdgcn_mfma_f32_32x32x16_bf16(__builtin_bit_cast(bf8, a), __builtin_bit_cast(bf8, b), c, 0, 0, 0); }
DEV f16v mfma32x8(u2 a, u2 b, f16v c) { return __builtin_amdgcn_mfma_f32_32x32x8bf16_1k(__builtin_bit_cast(s4, a), __builtin_bit_cast(s4, b), c, 0, 0, 0); }
DEV f4 mfma16x16(u2 a, u2 b, f4 c) { return __builtin_amdgcn_mfma_f32_16x16x16bf16_1k(__builtin_bit_cast(s4, a), __builtin_bit_cast(s4, b), c, 0, 0, 0); }
DEV int fmap(int fp) {
  return fp < 512 ? (fp >> 4) * 18 + ((fp >> 1) & 4) + (fp & 3) + (fp & 4) * 2 : ((fp - 512) >> 1) * 18 + 16 + (fp & 1);
}
template <int CTRL> DEV float dpp(float v) { return __int_as_float(__builtin_amdgcn_update_dpp(0, __float_as_int(v), CTRL, 15, 15, false)); }
DEV float rowmax16(float v) { v = fmaxf(v, dpp<0x128>(v)); v = fmaxf(v, dpp<0x124>(v)); v = fmaxf(v, dpp<0x122>(v)); return fmaxf(v, dpp<0x121>(v)); }
DEV f2 swap16(float v) { auto r = __builtin_amdgcn_permlane16_swap(__float_as_uint(v), __float_as_uint(v), false, false); return f2{__uint_as_float(r[0]), __uint_as_float(r[1])}; }
DEV f2 swap32(float v) { auto r = __builtin_amdgcn_permlane32_swap(__float_as_uint(v), __float_as_uint(v), false, false); return f2{__uint_as_float(r[0]), __uint_as_float(r[1])}; }
DEV f2 builtin_swap32(float a, float b) { auto r = __builtin_amdgcn_permlane32_swap(__float_as_uint(a), __float_as_uint(b), false, false); return f2{__uint_as_float(r[0]), __uint_as_float(r[1])}; }
typedef __amdgpu_buffer_rsrc_t rsrc_t;
constexpr int WT_ = 16;
DEV rsrc_t mkrs(const void* p, unsigned n) { return __builtin_amdgcn_make_buffer_rsrc((void*)p, 0, n, 0x00020000); }
template <int A = 0> DEV void st16(rsrc_t r, unsigned o, u4 v, unsigned so = 0) { __builtin_amdgcn_raw_buffer_store_b128(v, r, o, so, A); }
template <int A = 0> DEV void st8(rsrc_t r, unsigned o, u2 v) { __builtin_amdgcn_raw_buffer_store_b64(v, r, o, 0, A); }
template <int A = 0> DEV void st4(rsrc_t r, unsigned o, unsigned v) { __builtin_amdgcn_raw_buffer_store_b32(v, r, o, 0, A); }
template <int A = 0> DEV void st2(rsrc_t r, unsigned o, u16 v) { __builtin_amdgcn_raw_buffer_store_b16(v, r, o, 0, A); }
template <int A = 0> DEV u4 ld16(rsrc_t r, unsigned o, unsigned so = 0) { return __builtin_amdgcn_raw_buffer_load_b128(r, o, so, A); }
template <int A = 0> DEV u2 ld8(rsrc_t r, unsigned o, unsigned so = 0) { return __builtin_amdgcn_raw_buffer_load_b64(r, o, so, A); }
DEV u4 pub(u4 v) { return __builtin_elementwise_min(v, u4{~1u, ~1u, ~1u, ~1u}); }
DEV unsigned top(u4 v, unsigned m) { return max(max(m, max(v[0], v[1])), max(v[2], v[3])); }
constexpr u4 ONES = {~0u, ~0u, ~0u, ~0u};
DEV float sq2(unsigned u) { return bflo(u) * bflo(u) + bfhi(u) * bfhi(u); }
DEV float dot2(unsigned u, float c) { const bf2 e = __builtin_bit_cast(bf2, u); return __builtin_amdgcn_fdot2_f32_bf16(e, e, c, false); }
DEV float sum2(unsigned u, float c) { return __builtin_amdgcn_fdot2_f32_bf16(__builtin_bit_cast(bf2, u), __builtin_bit_cast(bf2, 0x3f803f80u), c, false); }
DEV void unzip(u4& a, u4& b) { const u4 x = {a[0], a[1], b[0], b[1]}, y = {a[2], a[3], b[2], b[3]}; a = x; b = y; }
DEV void unzip2(u4& a, u4& b) {
  unsigned p = a[2], q = b[0], r = a[3], t = b[1];
  asm volatile("v_swap_b32 %0, %1" : "+v"(p), "+v"(q));
  asm volatile("v_swap_b32 %0, %1\n\ts_nop 1" : "+v"(r), "+v"(t));
  a[2] = p; b[0] = q; a[3] = r; b[1] = t;
}
#define SB() __builtin_amdgcn_sched_barrier(0)
#define PIN(x) asm volatile("" : "+v"(x)); SB()

DEV float ssq(f4 a, int j) { return a[0] * a[0] + (j ? a[1] * a[1] : 0.f) + a[2] * a[2] + a[3] * a[3]; }
DEV float red16(float a, float c) { auto e = __builtin_amdgcn_permlane16_swap(__float_as_uint(a), __float_as_uint(c), false, false); return __uint_as_float(e[0]) + __uint_as_float(e[1]); }
template <int N, bool F = false, int NW = 9>
DEV void row_stats(char* scr, int w, int l, unsigned* cnt, rsrc_t rw, float q0, float q1, unsigned dst) {
  const f2 h = builtin_swap32(q0, q1);
  float* red = (float*)scr;
  if (l < N) red[w * N + l] = h[0] + h[1];
  asm volatile("" ::: "memory");
  unsigned n = 0;
  if (l == 0) n = __hip_atomic_fetch_add((LDS unsigned*)cnt, 1u, __ATOMIC_RELAXED, __HIP_MEMORY_SCOPE_WORKGROUP);
  if (__builtin_amdgcn_readfirstlane(n) == NW - 1u && l < N) {
    asm volatile("" ::: "memory");
    float q = 0;
#pragma unroll
    for (int i = 0; i < NW; i++) q += red[i * N + l];
    if (F) st4<WT_>(rw, dst, min(__float_as_uint(q) & 0x7fffffffu, 0x7f800000u));
    else st2<WT_>(rw, dst, (u16)min(pkbf(q, q) & 0x7fffu, 0x7f80u));
  }
}

template <bool EO>
DEV u4 fold8(u4 a, u4 c, unsigned pm) {
  float lo[4], hi[4];
#pragma unroll
  for (int i = 0; i < 4; i++) {
    const unsigned u = c[i];
    lo[i] = __builtin_amdgcn_fdot2_f32_bf16(__builtin_bit_cast(bf2, u), __builtin_bit_cast(bf2, pm >> 16), bflo(a[i]), false);
    hi[i] = __builtin_amdgcn_fdot2_f32_bf16(__builtin_bit_cast(bf2, u), __builtin_bit_cast(bf2, pm), bfhi(a[i]), false);
  }
  return EO ? u4{pkbf(lo[0], lo[1]), pkbf(lo[2], lo[3]), pkbf(hi[0], hi[1]), pkbf(hi[2], hi[3])} : u4{pkbf(lo[0], hi[0]), pkbf(lo[1], hi[1]), pkbf(lo[2], hi[2]), pkbf(lo[3], hi[3])};
}
DEV int slot_of(int e, int p) { return e < 16 ? e + 16 * p : e + 16 + 2 * p; }
DEV int swz(int r, int sl) { return (r ^ (sl & 1) ^ (sl & 6) * 2) * 16; }
DEV u4 pair16(f4 a, f4 c) {
  auto e0 = __builtin_amdgcn_permlane16_swap(pkbf(a[0], a[1]), pkbf(c[0], c[1]), false, false);
  auto e1 = __builtin_amdgcn_permlane16_swap(pkbf(a[2], a[3]), pkbf(c[2], c[3]), false, false);
  return u4{e0[0], e1[0], e0[1], e1[1]};
}
DEV u4 pack8(f4 a, f4 c) { return u4{pkbf(a[0], a[1]), pkbf(a[2], a[3]), pkbf(c[0], c[1]), pkbf(c[2], c[3])}; }
DEV void gen_r(u4 (&wf)[2][5], int j, int g, float sg) {
  const int c = j >= 2 ? (j & ~1) - (j < 288 ? 0 : 287) : 144 * j;
  const float K = 1.0f / 576.0f, cf = (float)c, n0 = (float)(c * (16 * g + 68) + (j >= 2 ? (j & 1) * 144 : 72 * j));
  const float a = cf * K, r0 = fmaf(floorf(n0 * K), -576.f, n0) * K, r1 = fmaf(floorf(cf * (64.f * K)), -576.f, cf * 64.f) * K;
  const float ca = 2.f * __builtin_amdgcn_cosf(4.f * a), ct = 2.f * __builtin_amdgcn_cosf(r1);
#pragma unroll
  for (int p = 0; p < 2; p++) {
    const float m = p ? (g < 2 ? 0.f : sg) : (g < 2 ? 1.f : 0.f);
    f2 x[2][4];
#pragma unroll
    for (int e = 0; e < 2; e++) {
      const float r = e ? r0 + r1 : r0;
      x[e][1] = f2{__builtin_amdgcn_cosf(p ? r + a : r), __builtin_amdgcn_cosf(fmaf(a, 2.f + p, r))};
      x[e][2] = f2{__builtin_amdgcn_cosf(fmaf(a, 4.f + p, r)), __builtin_amdgcn_cosf(fmaf(a, 6.f + p, r))};
      x[e][0] = x[e][1] * ca - x[e][2]; x[e][3] = x[e][2] * ca - x[e][1];
    }
#pragma unroll
    for (int i = 0; i < 4; i++) {
      f2 y[5];
      y[1] = x[0][i]; y[2] = x[1][i];
      y[0] = y[1] * ct - y[2]; y[3] = y[2] * ct - y[1];
      y[4] = (p ? y[0] * ct - y[1] : y[3] * ct - y[2]) * m;
#pragma unroll
      for (int t = 0; t < 5; t++) wf[p][t][i] = pkbf(y[t][0], y[t][1]);
    }
  }
#pragma unroll
  for (int t = 0; t < 5; t++) asm volatile("" : "+v"(wf[0][t]), "+v"(wf[1][t]));
  SB();
}
#define FW 7
#define FW1 0
#define QSL 32
#define TSL 6
#define XPZ 1
#define CW 15
constexpr float RSC = 0.05892556509887896f, RS0 = 1.0f / 24.0f;
constexpr int SPIN = 1 << 18;
#define WAITFL(M) _Pragma("unroll 1") for (int it = 0; it < SPIN && (rdy & (M)) != (M); it++) { if (it) __builtin_amdgcn_s_sleep(1); rdy = (unsigned)__ballot(fl[l & 31] != 0u); } asm volatile("" ::: "memory")

constexpr int CV_Z = 16384, CV_T = 36864, CV_M = CV_T + 34304, CV_END = CV_T + 34816;
constexpr int L_W = CHB, L_X = L_W + 64512, L_Q = 8 * CHB - 9408, L_P3 = 8 * CHB, L_CT = L_P3 + 4096, L_RS = L_CT + 512, L_ST = L_RS + 1024, L_O = L_X;
static_assert(L_X + 8 * 7168 <= L_Q && L_X + CV_END <= L_CT && L_ST + 1152 <= 163000, "");
#define BAR(W) asm volatile(W "s_barrier" ::: "memory")
#define PAD(P)
#define PAD0()
#define POI 0
#define PSL 0
typedef __attribute__((ext_vector_type(3))) unsigned u3;
DEV u4 rdtr(const LDS char* p) {
  const u2 a = __builtin_bit_cast(u2, __builtin_amdgcn_ds_read_tr16_b64_v4i16((LDS s4*)p)), c = __builtin_bit_cast(u2, __builtin_amdgcn_ds_read_tr16_b64_v4i16((LDS s4*)(p + 256)));
  return u4{a[0], a[1], c[0], c[1]};
}
DEV void fft_consts(char* smem, int p, int l) {
  const char* ft = (const char*)&FT_DEV;
  asm volatile("" : "+s"(ft));
  asm volatile("s_nop 0\n\tglobal_load_lds_dwordx4 %2, %0" :: "s"(ft + p), "{m0}"((unsigned)(uintptr_t)(LDS char*)smem + L_X + CV_T + p), "v"(l * 16) : "memory");
}

DEV void touch(char* smem, rsrc_t r, unsigned o, unsigned so) {
  asm volatile("s_nop 0\n\tbuffer_load_dword %0, %1, %2 offen sc1 lds" :: "v"(o), "s"(r), "s"(so), "{m0}"((unsigned)(uintptr_t)(LDS char*)smem + 163072) : "memory");
}
DEV unsigned epoch(char* ws) {
  const __attribute__((address_space(4))) unsigned* p = (const __attribute__((address_space(4))) unsigned*)(ws + O_EP);
  asm volatile("" : "+s"(p));
  return *p;
}

#define XP1 3
#define EPL 0
#define MSL 2
#define MAPQ 4
#define PD 4
#define ADP 3
#define TGO 0
#define PD3 0
#define QC 0
#define S0 16
#define R0 8
#define S1 16
#define R1 8
#define SM 16
#define RM 8
#define IW 1
#define GR 1
#define WU 3
#define WUA 16
#define FE 0
#define TG2 14
#define TGI 31
#define PW 0
#define PWD 0
#define PW0 64
#define PW1 WS_END
#define PA 0
#define PO 3
#define PWW 8
#define FPR 3
#define MPR 2
#define WL 0
#define OTC 0
#define WBL 0
#define SPR 3
#define RPF 4
#define FSL2 0
#define CFP 0
#define RSH 0
#define PRE 5
DEV int head_of(int b) { return 8 * ((b >> 1) & 3) + (b >> 5); }
DEV int band_of(int b) { return 4 * (b & 1) + ((b >> 3) & 3); }
DEV unsigned gate(int b, int l) {
  const int m = min(l, 11);
  return O_MW + (36 * (m >> 2) + ((18 * head_of(b) + 17 * (m & 1)) >> 5) * 2 + ((m >> 1) & 1)) * 128;
}
template <int SL = MSL>
DEV void wait_flags(rsrc_t rw, unsigned o, unsigned tag) {
  unsigned m = ~tag;
#pragma unroll 1
  for (int it = 0; it < SPIN; it++) {
    asm volatile("" ::: "memory");
    if (m != tag) m = __builtin_amdgcn_raw_buffer_load_b32(rw, o, 0, WT_);
    if (__all(m == tag)) break;
    __builtin_amdgcn_s_sleep(SL);
  }
}

DEV unsigned now() { return (unsigned)__builtin_amdgcn_s_memrealtime(); }
template <int SL = MSL>
DEV unsigned wait_timed(rsrc_t rw, unsigned o, unsigned tag) {
  u2 m = {~tag, 0u};
#pragma unroll 1
  for (int it = 0; it < SPIN; it++) {
    asm volatile("" ::: "memory");
    if (m[0] != tag) m = ld8<WT_>(rw, o);
    if (__all(m[0] == tag)) break;
    __builtin_amdgcn_s_sleep(SL);
  }
  return m[1];
}
DEV void until(unsigned t, unsigned dt) {
#pragma unroll 1
  for (int it = 0; it < 256 && __any(now() - t < dt); it++) {}
}

typedef unsigned long long u64;
struct Args { __attribute__((ext_vector_type(4))) u64 a; __attribute__((ext_vector_type(2))) u64 b; u64 c; };
#define GLB __attribute__((address_space(1)))
#define ARGS(P, Q) u64 p0, p1, p2; asm volatile("s_waitcnt lgkmcnt(0)\n\ts_mov_b64 %0, %3\n\ts_mov_b64 %1, %4\n\ts_mov_b64 %2, %5" : "=&s"(p0), "=&s"(p1), "=&s"(p2) : "s"(ka.a[0]), "s"(P), "s"(Q), "s"(ka.a), "s"(ka.b), "s"(ka.c)); \
  char* const ws = (char*)(GLB char*)p0
DEV unsigned first(char* smem, const Args& ka, int b, int w) {
  constexpr int F_CNT = 18432, F_WO = L_X, F_T = L_X, F_RED = L_X + 24576;
  const int l = lane_id(), tid = w * 64 + l, lr = l & 15, g = l >> 4;
  volatile LDS unsigned* fl = (volatile LDS unsigned*)(LDS char*)(smem + F_CNT);
  const bool cv = b < 144;
  const int x = b & 7, sh = (b >> 3) & 1, y = (b >> 4) - 9, mt = cv ? x * 8 + y + 9 : y < 6 ? 2 * x + 8 * (y & 6) + (y & 1) : 48 + x, m2 = x + 8 * y;
  const bool idle = cv ? b >= 128 : mt >= 54;
  const unsigned pm = 0x3f800000u | sh << 31;
  STAMP_DECL; STAMP(0);
  unsigned tag;
#define rw mkrs(ws, WS_END)
#define TAG() asm volatile("s_add_u32 %0, %0, 1" : "+s"(tag) :: "scc")
#define ODBG(i)
#define COUNT(I) unsigned n_ = 0; if (l == 0) n_ = __hip_atomic_fetch_add((LDS unsigned*)fl + (I), 1u, __ATOMIC_RELAXED, __HIP_MEMORY_SCOPE_WORKGROUP); n_ = __builtin_amdgcn_readfirstlane(n_)
#define OPEN() asm volatile("s_waitcnt vmcnt(0)" ::: "memory"); \
  { COUNT(31); if (n_ == 13u && l < R0) { st4<WT_>(rw, O_M3 + (l * 128 + b) * S0, tag); if (l == 0) { ODBG(0); } } } if (l == 0) { ODBG(1 + (w < 7) + (w < 3)); }
  if (w < 3) {
    __builtin_amdgcn_s_setprio(3);
    const int row = (tid * 171) >> 11, k0 = tid - 12 * row;
    unsigned o = cv ? ((mt >> 4) * 512 + (mt & 15) * 16 + row) * 1152 + k0 * 16 : (tid >> 2) * 3456 + mt * 64 + (tid & 3) * 16;
    const unsigned A = cv ? 256 * 1152 : 48 * 3456, B = cv ? 192 : 96 * 3456, H = cv ? 576 : 288 * 3456;
    asm volatile("" : "+v"(o));
    ARGS(ka.a[2], ka.b[1]);
    ASTAMP(3);
    const rsrc_t rs = mkrs((void*)(cv ? p1 : p2), idle ? 0 : cv ? ACT : 3 * WSQ);
    u4 st[12];
#pragma unroll
    for (int h = 0; h < 6; h++) { st[2 * h] = ld16(rs, o, (h & 1) * A + (h >> 1) * B); st[2 * h + 1] = ld16(rs, o, (h & 1) * A + (h >> 1) * B + H); }
    SB();
    ASTAMP(4);
    tag = epoch(ws);
    int q[4];
#pragma unroll
    for (int h = 0; h < 4; h++) {
      const int sx = slot_of((k0 >> 1) + 6 * min(h, 2), h == 3), sw = slot_of(h < 2 ? 3 * h + w : 15 + w, (l >> 2) & 1);
      q[h] = cv ? sx * 512 + swz(row, sx) + (k0 & 1) * 8 : sw * 512 + (l >> 3) * 64 + ((l & 3) ^ (sw & 1) * 2) * 16;
    }
    asm volatile("" : "+v"(q[0]), "+v"(q[1]), "+v"(q[2]), "+v"(q[3]));
    TAG();
    if (tid == 0) fl[32] = tag;
    SB();
    STAMP(1);
    if (cv) {
#pragma unroll
      for (int h = 0; h < 6; h++) {
        st[h] = fold8<true>(st[2 * h], st[2 * h + 1], pm);
        *(u2*)(smem + q[h >> 1] + (h & 1) * 256) = u2{st[h][0], st[h][1]};
        *(u2*)(smem + (h < 4 ? q[h >> 1] + 8192 : q[3]) + (h & 1) * 256) = u2{st[h][2], st[h][3]};
      }
      SB();
      f4 s = {0, 0, 0, 0};
#pragma unroll
      for (int h = 0; h < 24; h++) { s[h >> 1 & 2] = dot2(st[h >> 2][h & 3], s[h >> 1 & 2]); s[h >> 1 & 2 | 1] = sum2(st[h >> 2][h & 3], s[h >> 1 & 2 | 1]); }
      *(f4*)(smem + F_RED + tid * 16) = s;
    } else {
#pragma unroll
      for (int h = 0; h < 6; h++) *(u4*)(smem + (h < 5 ? q[h & 1] + (h >> 1) * 3072 : q[2])) = fold8<false>(st[2 * h], st[2 * h + 1], pm);
    }
    STAMP(2);
    __builtin_amdgcn_s_waitcnt(0x70);
    BAR("");
    if (cv) {
      __builtin_amdgcn_s_setprio(SPR);
      if (w < 2) {
#pragma unroll 1
        for (int it = 0; it < SPIN && !idle && fl[24] < TGO; it++) __builtin_amdgcn_s_sleep(2);
#pragma unroll 1
        for (int p = w; p < 34; p += 2) fft_consts(smem, p * 1024, l);
        asm volatile("s_waitcnt vmcnt(0)" ::: "memory");
        if (l == 0) asm volatile("ds_add_u32 %0, %1" :: "v"((LDS char*)smem + L_CT + 240), "v"(w + 1) : "memory");
      } else {
        f2 t = {0, 0};
#pragma unroll
        for (int i = 0; i < 12; i++) t += *(const f2*)(smem + F_RED + ((l >> 1 & 15) * 12 + i) * 16 + (l & 1) * 8);
        const float v = __uint_as_float(min(__float_as_uint(fmaxf(fmaf(sh ? 0.f : -t[1] * (1.f / 576.f), t[1], 0.5f * t[0]), 0.f)), 0x7f000000u)), hi = rbf(v);
        if (l < 32) {
          if (!idle) st4<WT_>(rw, O_SS + (mt * 32 + l) * 8 + 4 * sh, pkbf(hi, v - hi));
          st16<WT_>(mkrs(ws + O_SQ, 65536), b * 512 + l * 16, ONES);
        }
        OPEN();
      }
    }
  } else if (w < 7) {
    __builtin_amdgcn_s_setprio(3);
    if (b < 128) {
      const int r = (b >> 3) & 7, lg = x * 8, lane = lg + (l >> 3), c0 = ((b >> 6) * 4 + w - 3) * 36 + 1;
      unsigned src = ((8 * ((lane & 15) + 64 * (lane >> 4)) + r) * 289 + c0 + min(l & 7, 5) * 6) * 2;
      asm volatile("" : "+v"(src));
      ARGS(ka.a[3], ka.b[0]);
      tag = epoch(ws);
      __builtin_amdgcn_s_sleep(TSL);
      u3 fr[4], fi[4];
#pragma unroll
      for (int i = 0; i < 4; i++) {
        fr[i] = __builtin_amdgcn_raw_buffer_load_b96(mkrs((void*)p1, S * 578), src, i * 128 * 578, 0);
        fi[i] = __builtin_amdgcn_raw_buffer_load_b96(mkrs((void*)p2, S * 578), src, i * 128 * 578, 0);
      }
      SB();
      ASTAMP(4);
      if (WU && w == 3) touch(smem, rw, l < 32 ? O_SS + (mt * 32 + l) * 8 : O_M3 + ((l & (R0 - 1)) * 128 + b) * S0, 0);
      unsigned dst[5];
#pragma unroll
      for (int h = 0; h < 5; h++) {
        const int cc = 8 * h + (l >> 3), c = c0 + cc, P = c == 288 ? 0 : (c > 144 ? (432 - c) >> 1 : c >> 1) + (c & 1) * 144;
        dst[h] = cc < 36 ? O_WT + ((P * 8 + r) * 64 + lg + (l & 7)) * 16 : 1u << 31;
      }
      asm volatile("" : "+v"(dst[0]), "+v"(dst[1]), "+v"(dst[2]), "+v"(dst[3]), "+v"(dst[4]));
      TAG();
      __builtin_amdgcn_s_setprio(FPR);
      BAR("");
      if (FSL2) __builtin_amdgcn_s_sleep(FSL2);
      SB();
      STAMP(1);
      u4* t = (u4*)(smem + F_T + (w - 3) * 6144);
#pragma unroll
      for (int h = 0; h < 6; h++) {
        u4 v;
#pragma unroll
        for (int i = 0; i < 4; i++) v[i] = __builtin_amdgcn_perm(fi[i][h >> 1], fr[i][h >> 1], (h & 1) ? 0x07060302u : 0x05040100u);
        t[((l & 7) * 6 + h) * 8 + ((l >> 3) ^ (l & 7))] = v;
      }
      STAMP(2);
#pragma unroll
      for (int h = 0; h < 5; h++) {
        const int cc = 8 * h + (l >> 3);
        st16<WT_>(rw, dst[h], t[cc * 8 + ((l & 7) ^ ((cc * 43) >> 8))]);
      }
      STAMP(5);
      OPEN();
    } else {
      u4 st[6]; int q[3][2];
      ARGS(ka.c, ka.c);
      tag = epoch(ws);
      TAG();
      __builtin_amdgcn_s_sleep(TSL);
      const rsrc_t rs = mkrs((void*)p1, !cv && m2 < 36 && w < 6 ? WSQ : 0);
#pragma unroll
      for (int h = 0; h < 3; h++) {
        const int u = tid + 192 * (h - 1), row = (u * 1821) >> 16, kb = u - 36 * row;
        const unsigned o = fmap(m2 * 16 + row) * 1152 + kb * 16;
        st[2 * h] = ld16(rs, o); st[2 * h + 1] = ld16(rs, o + 576);
#pragma unroll
        for (int p = 0; p < 2; p++) { const int sl = slot_of(kb >> 1, p); q[h][p] = F_WO + sl * 256 + swz(row, sl) + (kb & 1) * 8; }
      }
      BAR("");
      SB();
      if (w < 6) {
#pragma unroll
        for (int h = 0; h < 3; h++) {
          const u4 v = fold8<true>(st[2 * h], st[2 * h + 1], pm);
          *(u2*)(smem + q[h][0]) = u2{v[0], v[1]}; *(u2*)(smem + q[h][1]) = u2{v[2], v[3]};
        }
        asm volatile("" ::: "memory");
        fl[w] = 1u;
      }
      __builtin_amdgcn_s_waitcnt(0xf70);
    }
  } else {
    const int j0 = sh * 288 + (w - 7) * 16;
    u4 wf[2][5], xf[9][2];
    if (w == 7 && l < 32) fl[l] = 0u;
    if (w == 8 && l < 32) *(u4*)(smem + L_CT + l * 16) = u4{0, 0, 0, 0};
    __builtin_amdgcn_s_setprio(2);
    gen_r(wf, j0 + lr, g, sh ? -1.f : 1.f);
    unsigned o;
    if (cv) o = O_XT0 + (((j0 >> 2) + g) * S + mt * 32 + 2 * lr) * 8;
    else {
      const int col = mt * 32 + (l & 31), hd = (col * 3641) >> 16, d = col - 18 * hd, sec = hd >> 5;
      const int pos = d < 16 || sec == 2 ? 16 * sec + d : 34 + 2 * sec + d;
      o = O_WQ2 + (((hd & 31) * 72 + (j0 >> 3) + (g >> 1)) * 56 + pos) * 16;
    }
    const LDS char* xa[2];
#pragma unroll
    for (int n = 0; n < 2; n++) xa[n] = (LDS char*)smem + g * 512 + (cv ? swz(lr, g + 4 * n) : ((g ^ n) & 1) * 32 + (lr >> 2) * 64 + (lr & 3) * 8);
    asm volatile("" : "+v"(o), "+v"(xa[0]), "+v"(xa[1]));
    ARGS(0ull, 0ull);
    STAMP(2);
    __builtin_amdgcn_s_setprio(MPR);
    BAR("");
    STAMP(3);
    const unsigned tv = fl[32];
    if (!idle) {
      f4 acc[2][2];
#define TT(u) ((u) == 8 ? 8 : ((u) >> 1) + ((u) & 1) * 4)
#define RMUL(RD)                                                                                                                    \
      _Pragma("unroll") for (int u = 0; u < RPF; u++) { xf[u][0] = RD(TT(u), 0); xf[u][1] = RD(TT(u), 1); }                          \
      SB();                                                                                                                         \
      _Pragma("unroll") for (int u = 0; u < 10; u++) {                                                                              \
        if (u + RPF < 9) { xf[u + RPF][0] = RD(TT(u + RPF), 0); xf[u + RPF][1] = RD(TT(u + RPF), 1); }                              \
        _Pragma("unroll") for (int n = 0; n < 2; n++) { acc[u & 1][n] = mfma16(wf[u & 1][u >> 1], xf[min(u, 8)][n], u > 1 ? acc[u & 1][n] : f4{0, 0, 0, 0}); PIN(acc[u & 1][n]); } \
      }                                                                                                                             \
      STAMP(4);                                                                                                                     \
      _Pragma("unroll") for (int n = 0; n < 2; n++) {                                                                               \
        const f4 e = acc[0][n], d = acc[1][n];                                                                                      \
        acc[0][n] = (e + d) * sc; acc[1][n] = (e - d) * f4{sc, -sc, sc, -sc};                                                       \
        if (j0 == 0 && g == 0) { acc[0][n][0] = (e[0] - d[0]) * (RS0 * qs); acc[0][n][1] = cv ? (e[0] + d[0]) * RS0 : 0.f; acc[1][n][0] = e[1] * (sc * RH); acc[1][n][1] = d[1] * (sc * RH); } \
      }
      constexpr float RH = 1.f / 0.70703125f;
      const float qs = !cv && mt < 18 ? 0.23570226039551584f * 1.4426950408889634f : 1.f, sc = RSC * qs;
      if (cv) {
#define RDX(t, n) *(const LDS u4*)(xa[(t) & 1] + (t) * 2048 + (n) * 256)
        RMUL(RDX);
        st16<WT_>(rw, o, pub(pack8(acc[0][0], acc[0][1]))); st16<WT_>(rw, o, pub(pack8(acc[1][0], acc[1][1])), 36 * S * 8);
        if (l == 0) asm volatile("ds_add_u32 %0, %1" :: "v"(fl + 24), "v"(1u) : "memory");
        tag = __builtin_amdgcn_readfirstlane(tv);
        STAMP(5);
        OPEN();
      } else {
#define RDW(t, n) rdtr(xa[n] + (t) * 2048)
        RMUL(RDW);
        st16<WT_>(rw, o, pair16(acc[0][0], acc[0][1])); st16<WT_>(rw, o, pair16(acc[1][0], acc[1][1]), 18 * 896);
        STAMP(5);
        asm volatile("s_waitcnt vmcnt(0)" ::: "memory");
        COUNT(31);
        if (n_ == 8u && l == 0) st4<WT_>(rw, O_MW + (2 * mt + sh) * 128, tv);
        STAMP(6);
        if (m2 < 36) {
          __builtin_amdgcn_s_setprio(0);
#pragma unroll 1
          for (int it = 0; it < SPIN && !__all(fl[3 + min(l, 2)] != 0u); it++) __builtin_amdgcn_s_sleep(1);
          asm volatile("" ::: "memory");
#pragma unroll
          for (int t = 0; t < 9; t++) xf[t][0] = *(const u4*)(smem + F_WO + g * 256 + swz(lr, g + 4 * (t & 1)) + t * 1024);
          SB();
#pragma unroll
          for (int u = 0; u < 10; u++) acc[u & 1][0] = mfma16(xf[u < 8 ? TT(u) : 8][0], wf[u & 1][u >> 1], u > 1 ? acc[u & 1][0] : f4{0, 0, 0, 0});
          const f4 e = acc[0][0], d = acc[1][0];
          f4 p = (e + d) * RSC, m = (e - d) * (lr & 1 ? -RSC : RSC);
          if (j0 == 0) {
#pragma unroll
            for (int i = 0; i < 4; i++) {
              const float p0 = dpp<0xb1>(p[i]), e1 = dpp<0xb1>(e[i]);
              p[i] = lr == 0 ? m[i] * (RS0 / RSC) : lr == 1 ? p0 * (RS0 / RSC) : p[i];
              m[i] = lr == 0 ? e1 * (RSC * RH) : lr == 1 ? d[i] * (RSC * RH) : m[i];
            }
          }
          st16<WT_>(rw, O_WO2 + ((m2 * 2 + (g >> 1)) * 576 + j0 + lr + 144 * (g & 1)) * 16, pair16(p, m));
        }
      }
    }
    tag = __builtin_amdgcn_readfirstlane(tv);
  }
  STAMP_OUT(0, FW); STAMP_OUT(9, FW1);
#undef rw
  return tag;
}

DEV unsigned cmulh(unsigned a, unsigned t) {
  const h2 x = __builtin_bit_cast(h2, a), y = __builtin_bit_cast(h2, t);
  return __builtin_bit_cast(unsigned, __builtin_elementwise_fma(h2{x[0], x[0]}, y, h2{-x[1], x[1]} * h2{y[1], y[0]}));
}
DEV unsigned rot1(unsigned a) { unsigned r; asm("v_pk_mul_f16 %0, %1, 1.0 op_sel:[1,0] op_sel_hi:[0,0] neg_lo:[1,0]" : "=v"(r) : "v"(a)); return r; }
DEV float amax3(float a, float b, float c) { float r; asm("v_max3_f32 %0, |%1|, |%2|, |%3|" : "=v"(r) : "v"(a), "v"(b), "v"(c)); return r; }
template <int CTRL> DEV unsigned umaxd(unsigned v) { return max(v, (unsigned)__builtin_amdgcn_update_dpp(0, v, CTRL, 15, 15, true)); }
DEV u4 rot4(u4 a) { return u4{rot1(a[0]), rot1(a[1]), rot1(a[2]), rot1(a[3])}; }
DEV void fft256(u4 d, u4 Pre, u4 Pim, u4 T, u4 Qre, u4 Qim, f4& ore, f4& oim) {
  const f4 z = {0, 0, 0, 0};
  const f4 re = mfma16h(d, Pre, z), im = mfma16h(d, Pim, z);
  const u4 e = {cmulh(pkh(re[0], im[0]), T[0]), cmulh(pkh(re[1], im[1]), T[1]), cmulh(pkh(re[2], im[2]), T[2]), cmulh(pkh(re[3], im[3]), T[3])};
  ore = mfma16h(Qre, e, z); oim = mfma16h(Qim, e, z);
}
DEV u4 xperm(u4 v, int x) {
  if (x & 1) v = u4{v[1], v[0], v[3], v[2]};
  if (x & 2) v = u4{v[2], v[3], v[0], v[1]};
  return v;
}
#define CLATE 2
#define CVW 0
#define M1P 0
#define M3D 0
#define CSL 2
#define PE 2
#define WLA 0
#define M3T 130
#define M3P 4
#define CSTAMP(i)
#define DSTAMP(i) STAMP(i)
#define GOS "s_sleep 1\n\t"
#define CIP 4
#define CPR 1
template <int N>
DEV void fetch_w(char* smem, char* ws, int head, int k0, int l) {
  const unsigned lb = (unsigned)(uintptr_t)(LDS char*)smem;
#pragma unroll
  for (int i = 0; i < N; i++) {
    const int p = min(k0 + 64 / N * i, 62) * 1024;
    asm volatile("s_nop 0\n\tglobal_load_lds_dwordx4 %2, %0 sc1" :: "s"(ws + O_WQ2 + head * 64512 + p), "{m0}"(lb + L_W + p), "v"(l * 16) : "memory");
  }
}
DEV bool colconv(char* smem, char* ws, int b, int L, int w, int l, unsigned tag TPAR) {
  const int tid = w * 64 + l, lr = l & 15, g = l >> 4;
  const int cw = w >> 3, r = w & 7;
  const rsrc_t rw = mkrs(ws, WS_END);
  unsigned ro = (16 * w + lr + 256 * g) * 16, wo = tid * 16;
  u4 one = ONES;
  unsigned si = L ? O_SS1 : O_SS, xi = (L ? O_XT2 : O_XT0) + b * S * 8, xo = (L ? O_XT3 : O_XT1) + b * S * 8, fo = O_WT + b * 16384, xp = xo | L << 31;
  u4 ssp, v, wv;
  const int ci = L && w >= 12 ? (w ^ head_of(b) >> 3) & 3 : 0;
#define CLD() v = ld16<WT_>(rw, ro, xi); ssp = ld16<WT_>(rw, ro, si)
  volatile LDS unsigned* cf = (volatile LDS unsigned*)(LDS char*)(smem - L_X + L_CT + L * 256 + 240);
  unsigned st, tv;
#define BLD(D, O, SO) "buffer_load_dwordx4 %[" D "], %[" O "], %[rs], %[" SO "] offen sc1\n\t"
#define RDS(D) "ds_read_b32 %[tv], %[cf]\n\ts_waitcnt lgkmcnt(0)\n\tv_readfirstlane_b32 %[" D "], %[tv]\n\t"
  __builtin_amdgcn_s_setprio(CPR);
  CSTAMP(3);
  asm volatile("s_cmp_eq_u32 %[L], 0\n\ts_cbranch_scc1 1f\n\t" BLD("wv", "wo", "fo") "\n1:\n\t" RDS("st") "s_cmp_lt_u32 %[st], 64\n\ts_cbranch_scc1 2f\n\t" BLD("v", "ro", "xi") BLD("ss", "ro", "si") "\n2:"
               : [wv] "=&v"(wv), [v] "=&v"(v), [ss] "=&v"(ssp), [st] "=&s"(st), [tv] "=&v"(tv)
               : [L] "s"(L), [wo] "v"(wo), [ro] "v"(ro), [cf] "v"(cf), [rs] "s"(rw), [fo] "s"(fo), [xi] "s"(xi), [si] "s"(si) : "scc", "memory");
  if (ci) {
#pragma unroll 1
    for (int p = ci - 1; p < 34; p += 3) fft_consts(smem - L_X, p * 1024, l);
    asm volatile("s_waitcnt vmcnt(0)" ::: "memory");
    if (l == 0) asm volatile("ds_add_u32 %0, %1" :: "v"(cf), "v"(1u) : "memory");
  }
  CSTAMP(1);
  if ((st & 3u) != 3u) {
#pragma unroll 1
    for (int it = 0; it < SPIN && (*cf & 3u) != 3u; it++) __builtin_amdgcn_s_sleep(CSL);
  }
  asm volatile("" ::: "memory");
  const char* tp = smem + CV_T + l * 16;
  u4 F1re = *(const u4*)(tp + r * 4096), Tf = *(const u4*)(tp + r * 4096 + 1024), Ti = *(const u4*)(tp + r * 4096 + 2048), Gre = *(const u4*)(tp + r * 4096 + 3072);
  u4 Fre = *(const u4*)(tp + 32768);
  const u2 a8 = *(const u2*)(smem + CV_T + 33792 + l * 8);
  u4 F1im = rot4(F1re), Gim = rot4(Gre), Fim = rot4(Fre);
  u4 A0 = {a8[0], 0, a8[1], 0}, A1 = {0, a8[0], 0, a8[1]};
  u4 B0 = xperm(A0, w >> 2), B1 = xperm(A1, w >> 2);
  LDS char* yp = (LDS char*)smem + g * 2048 + ((w >> 2) * 64 + lr * 4 + ((w & 3) ^ g)) * 4;
  LDS char* zp = (LDS char*)smem + CV_Z + g * 4096 + (r >> 1) * 256 + lr * 16 + ((((r & 1) * 2 + cw) ^ g) * 4);
  LDS char* dp = (LDS char*)smem + w * 1024 + l * 16;
  asm volatile("" : "+v"(F1re), "+v"(F1im), "+v"(Tf), "+v"(Ti), "+v"(Gre), "+v"(Gim), "+v"(Fre), "+v"(Fim), "+v"(A0), "+v"(A1), "+v"(B0), "+v"(B1), "+v"(yp), "+v"(zp), "+v"(dp), "+v"(ro), "+v"(wo), "+v"(one), "+s"(si), "+s"(xi), "+s"(xo), "+s"(fo), "+s"(xp));
  SB();
  CSTAMP(2);
  if (L ? w > 8 && !ci : w < 3) {
    if (PD3 > 0 && L) __builtin_amdgcn_s_sleep(PD3);
    if (!L && b >= 128) {
      if (IW == 1) wait_flags(rw, O_M3 + ((b >> RSH & (R0 - 1)) * 128 + b - 128) * S0, tag);
      else if (IW > 1) __builtin_amdgcn_s_sleep(IW);
    } else if (!L && TG2) {
#pragma unroll 1
      for (int it = 0; it < SPIN && *(volatile LDS unsigned*)(LDS char*)(smem - L_X + 18432 + TGI * 4) < TG2; it++) __builtin_amdgcn_s_sleep(2);
      __builtin_amdgcn_s_sleep(PD);
    }
    const unsigned t = wait_timed(rw, w == 2 ? gate(b, l) : O_M3 + (L ? 32768 + ((b >> RSH & (R1 - 1)) * 256 + (w < 12 ? w - 8 : 0) * 64 + l) * S1 : ((b >> RSH & (R0 - 1)) * 128 + tid) * S0), tag);
    CSTAMP(4);
    if (L) until(t, M3T);
    if (l == 0) asm volatile("ds_add_u32 %0, %1" :: "v"(cf), "v"(16u) : "memory");
  }
  STAMP(0);
  __builtin_amdgcn_s_setprio(0);
  {
    unsigned n = SPIN;
    asm volatile("s_cmp_ge_u32 %[st], 64\n\ts_cbranch_scc1 4f\n\ts_branch 2f\n1:\n\t" GOS "s_sub_u32 %[n], %[n], 1\n\ts_cbranch_scc1 3f\n2:\n\t" RDS("st") "s_cmp_lt_u32 %[st], %[go]\n\ts_cbranch_scc1 1b\n3:\n\t"
                 "s_cmp_lg_u32 %[L], 0\n\ts_cbranch_scc1 5f\n\t" BLD("wv", "wo", "fo") "\n5:\n\t" BLD("v", "ro", "xi") BLD("ss", "ro", "si") "\n4:\n\t"
                 "buffer_store_dwordx4 %[one], %[ro], %[rs], %[xp] offen sc1\n\ts_barrier\n\ts_waitcnt vmcnt(3)"
                 : [wv] "+v"(wv), [v] "+v"(v), [ss] "+v"(ssp), [st] "+s"(st), [tv] "=&v"(tv), [n] "+s"(n)
                 : [L] "s"(L), [go] "s"(48 + 16 * L), [wo] "v"(wo), [ro] "v"(ro), [cf] "v"(cf), [rs] "s"(rw), [fo] "s"(fo), [xi] "s"(xi), [si] "s"(si), [one] "v"(one), [xp] "s"(xp) : "scc", "memory");
  }
  STAMP(6);
  f4 wr = {bflo(wv[0]), bflo(wv[1]), bflo(wv[2]), bflo(wv[3])}, wi = {bfhi(wv[0]), bfhi(wv[1]), bfhi(wv[2]), bfhi(wv[3])};
  {
    unsigned m = min(__float_as_uint(amax3(amax3(wr[0], wr[1], wr[2]), amax3(wi[0], wi[1], wi[2]), amax3(wr[3], wi[3], wi[3]))), 0x7e000000u);
    m = umaxd<0x121>(umaxd<0x122>(umaxd<0x124>(umaxd<0x128>(m))));
    if (lr == 0) asm volatile("ds_max_u32 %0, %1" :: "v"((LDS char*)smem + CV_M + cw * 4), "v"(m) : "memory");
  }
  asm volatile("s_waitcnt vmcnt(1)" : "+v"(wr), "+v"(wi), "+v"(v), "+v"(ssp));
  SB();
  float q0, q1;
#define COK() (q0 = sum2(ssp[0], sum2(ssp[1], 576e-5f)), q1 = sum2(ssp[2], sum2(ssp[3], 576e-5f)), top(v, 0u) != ~0u && !__builtin_isunordered(q0, q1))
  if (!COK()) {
    int it = 0;
#pragma unroll 1
    do { asm volatile("" ::: "memory"); CLD(); } while (!COK() && ++it < SPIN);
  }
  DSTAMP(1);
  const unsigned hm = b ? 0xffff0000u : 0u;
  const f4 z = {0, 0, 0, 0};
  const f4 xv[2] = {f4{bflo(v[0]), __uint_as_float(v[0] & hm), bflo(v[1]), bfhi(v[1])}, f4{bflo(v[2]), __uint_as_float(v[2] & hm), bflo(v[3]), bfhi(v[3])}};
  {
    const f4 u0 = xv[0] * __builtin_amdgcn_rsqf(q0), u1 = xv[1] * __builtin_amdgcn_rsqf(q1);
    const u4 d = {pkh(u0[0], u0[1]), pkh(u0[2], u0[3]), pkh(u1[0], u1[1]), pkh(u1[2], u1[3])};
    const f4 y0 = mfma16h(A0, d, z), y1 = mfma16h(A1, d, z);
    *(LDS unsigned*)yp = pkh(y0[0], y0[1]); *(LDS unsigned*)(yp + 1024) = pkh(y0[2], y0[3]);
    *(LDS unsigned*)(yp + 8192) = pkh(y1[0], y1[1]); *(LDS unsigned*)(yp + 9216) = pkh(y1[2], y1[3]);
  }
  if (!L) fetch_w<4>(smem - L_X, ws, head_of(b), w, l);
  DSTAMP(2);
  BAR("s_waitcnt vmcnt(4) lgkmcnt(0)\n\t");
  DSTAMP(3);
  if (tid == 0 && b < 8) st4<WT_>(rw, O_M1 + (288 + L * 8 + b) * 128, tag);
  if (w == 15 && l < RM) st4<WT_>(rw, O_M1 + L * 18432 + (l * 144 + b) * SM, tag);
  float unsc[2];
  {
    u4 d = *(const LDS u4*)dp;
    const u2 mx = *(const u2*)(smem + CV_M);
    const unsigned E0 = max((unsigned)__builtin_amdgcn_readfirstlane(mx[0]) >> 23, 1u), E1 = max((unsigned)__builtin_amdgcn_readfirstlane(mx[1]) >> 23, 1u);
    unsc[0] = __uint_as_float((E0 + 2) << 23); unsc[1] = __uint_as_float((E1 + 2) << 23);
    const float sc = __uint_as_float((253u - (cw ? E1 : E0)) << 23);
    f4 re, im;
    fft256(d, F1re, F1im, Tf, Fre, Fim, re, im);
    wr *= sc; wi *= sc;
    const f4 pr = re * wr - im * wi, pi = -(re * wi) - im * wr;
    d = u4{pkh(pr[0], pi[0]), pkh(pr[1], pi[1]), pkh(pr[2], pi[2]), pkh(pr[3], pi[3])};
    fft256(d, Fre, Fim, Ti, Gre, Gim, re, im);
#pragma unroll
    for (int i = 0; i < 4; i++) *(LDS unsigned*)(zp + i * 1024) = pkh(re[i], im[i]);
  }
  DSTAMP(4);
  BAR("s_waitcnt lgkmcnt(0)\n\t");
  STAMP(5);
  unsigned pm = ~tag;
  {
    const u4 d = *(const LDS u4*)(dp + CV_Z);
    const f4 y0 = mfma16h(B0, d, z), y1 = mfma16h(B1, d, z);
    const f4 us = {unsc[0], -unsc[0], unsc[1], -unsc[1]};
    u4 o = pack8(xv[0] + f4{y0[0], y0[1], y1[0], y1[1]} * us, xv[1] + f4{y0[2], y0[3], y1[2], y1[3]} * us);
    if (!b) { o[0] = (o[0] & 0xffffu) | (v[0] & 0xffff0000u); o[2] = (o[2] & 0xffffu) | (v[2] & 0xffff0000u); }
    o = pub(o);
    st16<WT_>(rw, ro, o, xo);
  }
  return EPL && w < 3 && __all(pm == tag);
}

#define NOGRAM 0
#define QE 0
#define OUTA 0
#define WPD 6
#define WSTAMP 7
#define XPRE 2
#define GSL 1
#define QD 0
#define XW 12
#define RCP 0
#define RSL 4
#define SSL 2
#define QP 0x2222
#define FP0 2
#define FKS 32
#define KSL 2
#define KP0 3
#define KP1 2
#define OPW 3
#define SPL 0
#define RUO 0
#define RUK 0
#define FLP 0
#define ATT_BOUND 64.0f
#define EXP2(x) __builtin_amdgcn_exp2f(x)
#define PKP(a, b) pkbf(a, b)
#define ATT_E(P, j, S_, o_) P[j] = PKP(EXP2(S_[o_]), EXP2(S_[o_ + 1])); P[j + 1] = PKP(EXP2(S_[o_ + 2]), EXP2(S_[o_ + 3])); PIN(P)
#define GLDS(G, L_, MOD) asm volatile("s_nop 0\n\tglobal_load_lds_dwordx4 %2, %0" MOD :: "s"(G), "{m0}"(L_), "v"(lo16) : "memory")
#define KV_FETCH(sl, j) GLDS(gkv + (qt ^ (sl)) * CHB + pc[j], lb + (sl) * CHB + pc[j], " sc1")
#define SLOT_A(QT, V1A, KA, TA, F, C)                                       \
  sb = mfma32x8(kt, QT, z); PIN(sb);                                        \
  TA; ATT_E(p0, 0, sa, 0);                                                  \
  if (C) { ob = mfma32(v1, p1, ob); PIN(ob); }                              \
  v1 = *(const LDS u4*)(vp + (V1A)); ATT_E(p0, 2, sa, 4);                       \
  sb = mfma32(km, qm[1], sb); PIN(sb);                                      \
  km = *(const LDS u4*)(kp + (KA)); ATT_E(p1, 0, sa, 8);                        \
  oa = mfma32(v0, p0, oa); PIN(oa);                                         \
  ATT_E(p1, 2, sa, 12); F;
#define SLOT_B(QT, V0A, F)                                                  \
  sa = mfma32x8(kt, QT, z); PIN(sa);                                        \
  ATT_E(p0, 0, sb, 0);                                                      \
  oa = mfma32(v1, p1, oa); PIN(oa);                                         \
  ATT_E(p0, 2, sb, 4);                                                      \
  sa = mfma32(km, qm[0], sa); PIN(sa);                                      \
  ATT_E(p1, 0, sb, 8);                                                      \
  ob = mfma32(v0, p0, ob); PIN(ob);                                         \
  v0 = *(const LDS u4*)(vp + (V0A)); ATT_E(p1, 2, sb, 12); F;
constexpr int VP = 352, OTB = 36864;
DEV u2 tail(const LDS char* p) { return u2{*(const LDS u16*)p, *(const LDS u16*)(p + 16)}; }
#define ATT_LO(B, C, F0, F1) SLOT_A(qt0[1], B * CHB + 2 * VP, B * CHB + 512, (void)0, F0, C) SLOT_B(qt1[0], B * CHB + 4 * VP, F1)
#define ATT_HI(B, N, F0, F1) SLOT_A(qt1[1], B * CHB + 6 * VP, N * CHB, kt = tail(tp + N * CHB), F0, 1) SLOT_B(qt0[0], N * CHB, F1)
#define ATT_PRO(B) kt = tail(tp + B * CHB); km = *(const LDS u4*)(kp + B * CHB); v0 = *(const LDS u4*)(vp + B * CHB); sa = mfma32x8(kt, qt0[0], z); sa = mfma32(km, qm[0], sa); PIN(sa)
#define ATT_ADV(N) kp += N * CHB; tp += N * CHB; vp += N * CHB; asm volatile("" : "+v"(kp), "+v"(tp), "+v"(vp))
#define NOP (void)0
#define QSTAMP(i)
#define TAIL 0
#define TSTAMP(i)
#define USTAMP(i)

DEV float add1(float a, float b) { asm("v_add_f32 %0, %0, %1" : "+v"(a) : "v"(b)); return a; }
DEV float sub1(float a, float b) { float r; asm("v_sub_f32 %0, %1, %2" : "=v"(r) : "v"(a), "v"(b)); return r; }
DEV u4 cat(u2 a, u2 c) { return u4{a[0], a[1], c[0], c[1]}; }
#define GPF 4
#define GSP 0
#define GSTAMP(i)
#define HP 3
#define HS 32
#define HSL 2
#define HD 0
#define FSL 3
#define M3W 7
#define NOX 0
#define XPR -1
#define XPQ 0
DEV float addmul(float a, float b, float c) {
  asm("v_add_f32 %0, %0, %1" : "+v"(b) : "v"(a));
  asm("v_mul_f32 %0, %0, %1" : "+v"(b) : "v"(c));
  return b;
}
DEV void recheck(u4& a, u4& c, rsrc_t r, unsigned o, unsigned so) {
  unsigned n, t = top(a, top(c, 0u));
  asm volatile(
      "v_cmp_eq_u32 vcc, -1, %2\n\t"
      "s_cbranch_vccz 2f\n\t"
      "s_mov_b32 %3, 0x40000\n\t"
      "v_add_u32 %2, 0x10000, %4\n"
      "1:\n\t"
      "buffer_load_dwordx4 v[120:123], %4, %5, %6 offen sc1\n\tbuffer_load_dwordx4 v[124:127], %2, %5, %6 offen sc1\n\t"
      "s_waitcnt vmcnt(0)\n\t"
      "v_max3_u32 v120, v120, v121, v122\n\tv_max3_u32 v124, v124, v125, v126\n\tv_max3_u32 v120, v120, v123, v127\n\tv_max_u32 v120, v120, v124\n\t"
      "v_cmp_eq_u32 vcc, -1, v120\n\t"
      "buffer_load_dwordx4 %0, %4, %5, %6 offen sc1\n\tbuffer_load_dwordx4 %1, %2, %5, %6 offen sc1\n\t"
      "s_sub_u32 %3, %3, 1\n\t"
      "s_cbranch_scc1 3f\n\t"
      "s_cbranch_vccnz 1b\n"
      "3:\n\t"
      "s_waitcnt vmcnt(0)\n"
      "2:"
      : "+v"(a), "+v"(c), "+v"(t), "=&s"(n) : "v"(o), "s"(r), "s"(so)
      : "vcc", "scc", "v120", "v121", "v122", "v123", "v124", "v125", "v126", "v127");
}
DEV void layer(char* smem, char* ws, u16* out, int b, int L, int w, unsigned tag, bool seen) {
#define LANES() l = lane_id(); tid = w * 64 + l; lr = l & 15; g = l >> 4; r = l & 31; hh = l >> 5; lo16 = l * 16
  int l, tid, lr, g, r, hh, lo16;
  LANES();
  const int head = head_of(b), qt = band_of(b);
  const int qg = w & 3, kq = w >> 2;
  const unsigned lb = (unsigned)(uintptr_t)(LDS char*)smem;
  const rsrc_t rw = mkrs(ws, WS_END);
  const unsigned XIN = L ? O_XT3 : O_XT1, O_O = O_X + (RUO ? 0 : L) * XCH, O_KV = O_X + (RUK ? 0 : L) * XCH + ACT, O_F = O_FK + L * 32768;
  unsigned* const ct = (unsigned*)(smem + L_CT + L * 256);
  STAMP_DECL; STAMP(0);
  __builtin_amdgcn_s_setprio(0);
#define RGM w
  const int rg = (RGM) & 7, kh = w >> 3;
  unsigned qx = (g * S + qt * 256 + rg * 32 + 2 * lr) * 8, qw = lb + (kh * 36 + hh) * 896 + r * 16, wt = qw + (r < 22 ? 512 : 256);
  unsigned a0 = lb + L_X + rg * 7168 + lo16, a1 = lb + L_X + rg * 7168 + 4096 + (hh * 24 + min(r, 23)) * 16;
  asm volatile("" : "+v"(qx), "+v"(qw), "+v"(wt), "+v"(a0), "+v"(a1));
  if (b < 144) __builtin_amdgcn_s_sleep(QC);
  else if (w < (ADP ? 3 : 1) || (w >= 12 && !L)) {
    wait_flags(rw, w >= 12 ? gate(b, l) : O_M1 + (288 + L * 8 + (b & 7)) * 128, tag);
    STAMP(1);
    if (w >= 12) fetch_w<16>(smem, ws, head, qg, l);
    else __builtin_amdgcn_s_sleep(QD);
  }
  if (w < 3 && !seen) wait_flags(rw, O_M1 + L * 18432 + ((b >> RSH & (RM - 1)) * 144 + w * 48 + min(l, 47)) * SM, tag);
  PAD(1 + 4 * L)
  if (kh) __builtin_amdgcn_s_setprio(3);
  if (b >= 144) asm volatile("s_waitcnt vmcnt(0)" ::: "memory");
  BAR("s_waitcnt vmcnt(1)\n\t");
  STAMP(0);
  {
    if (kh) {
      const unsigned sp = (g * S + qt * 256 + (head & 7) * 32 + 2 * lr) * 8, so = XIN + ((head >> 3) * 36 + 4 * (w - 8)) * S * 8, s2 = w == 15 ? so + 4 * S * 8 : 1u << 31;
      u4 v = ld16<WT_>(rw, sp, so), u = ld16<WT_>(rw, sp, s2);
#pragma unroll 1
      for (int it = 0; it < SPIN && __any(top(v, top(u, 0u)) == ~0u); it++) { asm volatile("" ::: "memory"); v = ld16<WT_>(rw, sp, so); u = ld16<WT_>(rw, sp, s2); }
      if (head < 8 && tid < 528) { v[0] &= 0xffffu; v[2] &= 0xffffu; }
      const float q = red16(dot2(v[0], dot2(v[1], dot2(u[0], dot2(u[1], 0.f)))), dot2(v[2], dot2(v[3], dot2(u[2], dot2(u[3], 0.f)))));
      row_stats<32, true, 8>(smem + L_ST, w - 8, l, ct + 13, rw, q, q, O_SQ + ((L * 4 + (head >> 3)) * S + qt * 256 + (head & 7) * 32 + l) * 4);
      if (tid == 768 && b >= 144) st4<WT_>(rw, O_M2 + (L * 128 + b - 144) * 32, tag);
    }
    {
      unsigned so = XIN + kh * 72 * S * 8, n = SPIN, t0, t1;
      if (kq == 0) __builtin_amdgcn_s_setprio(QP & 3); else if (kq == 1) __builtin_amdgcn_s_setprio(QP >> 4 & 3); else if (kq == 2) __builtin_amdgcn_s_setprio(QP >> 8 & 3); else __builtin_amdgcn_s_setprio(QP >> 12);
      STAMP(1);
#define QM
      asm volatile(QKV_ASM : [so] "+s"(so), [n] "+s"(n), [t0] "=&s"(t0), [t1] "=&s"(t1) QM
                   : [xp] "v"(qx), [wp] "v"(qw), [wt] "v"(wt), [a0] "v"(a0), [a1] "v"(a1), [rs] "s"(rw), [rg] "s"(rg), [w] "s"(w), [kv] "s"(O_KV + (head * 8 + qt) * CHB), [ct] "s"(lb + L_CT + L * 256)
                   : "vcc", "scc", "memory", QKV_CLOB);
    }
    STAMP(2);
    LANES();
    if (w < 4) {
      float q;
#pragma unroll 1
      for (int it = 0; it < SPIN; it++) {
        asm volatile("" ::: "memory");
        const f4 t = __builtin_bit_cast(f4, ld16<WT_>(rw, (g * S + qt * 256 + w * 64 + 4 * lr) * 4, O_SQ + L * 32768));
        const unsigned m = __builtin_amdgcn_raw_buffer_load_b32(rw, min(l, 27) * 32, O_M2 + L * 4096 + w * 896, WT_);
        const f2 h = builtin_swap32(red16(t[0], t[1]), red16(t[2], t[3]));
        q = h[0] + h[1];
        if (!__any(q != q || m != tag)) break;
        __builtin_amdgcn_s_sleep(SSL);
      }
      __builtin_amdgcn_s_waitcnt(0x70);
      *(float*)(smem + L_RS + (w * 64 + 4 * lr + g) * 4) = ln_rstd(q);
      if (l == 0) asm volatile("ds_add_u32 %0, %1" :: "v"((LDS unsigned*)ct + 32), "v"(1u) : "memory");
      __builtin_amdgcn_s_setprio(0);
      *(u4*)(smem + L_P3 + tid * 16) = u4{0, 0, 0, 0};
      if (w == 1) { const unsigned e = (l & 1) ? 0u : 0x3f803f80u; *(u4*)(smem + 8192 + (l >> 1) * VP + 288 + (l & 1) * 16) = u4{e, e, e, e}; }
    }
  }
  int pc[5];
#pragma unroll
  for (int j = 0; j < 5; j++) pc[j] = min(qg + 4 * j, 18) * 1024;
  const char* gkv = ws + O_KV + head * 8 * CHB;
  const char* fp = ws + O_F + head * 8 * FKS;
  const int r8 = (r & 3) * 2 + (r & 8), koff = hh * 4096 + r * 16, toff = 8512 + (hh * 4 + (r >> 4) * 2 + ((r >> 2) & 1)) * VP + r8, voff = 8192 + hh * VP + (r < 19 ? r * 16 : r < 20 || r >= 28 ? 304 : 304 + VP);
  const LDS char* kp = (LDS char*)smem + koff + kq * 1024;
  const LDS char* tp = (LDS char*)smem + toff + kq * 8 * VP;
  const LDS char* vp = (LDS char*)smem + voff + kq * 8 * VP;
  const int qo = L_Q + 8192 + qg * 128 + (r >> 4) * 2 * VP + (r & 4) * 8 + r8;
  const LDS char* qp = (LDS char*)smem + L_Q + hh * 4096 + (qg * 64 + r) * 16, *q0 = (LDS char*)smem + (hh ? L_P3 : qo), *q1 = (LDS char*)smem + (hh ? qo : L_P3);
  f16v oa = {0}, ob = {0};
  asm volatile("" : "+v"(kp), "+v"(tp), "+v"(vp), "+v"(qp), "+v"(q0), "+v"(q1), "+v"(lo16));
  if (kq == 1) __builtin_amdgcn_s_setprio(KP1); else if (kq == 0) __builtin_amdgcn_s_setprio(KP0); else __builtin_amdgcn_s_setprio(3);
  PAD(2 + 4 * L)
  QSTAMP(6);
  if (w < QKV_BW) BAR("s_waitcnt lgkmcnt(0)\n\t");
  STAMP(3);
#define POLL(f, IDX) _Pragma("unroll 1") for (int it = 0; it < SPIN; it++) {                                              \
    asm volatile("global_load_dwordx2 %0, %1, %2 sc1\n\ts_waitcnt vmcnt(0)" : "=v"(f) : "v"((IDX) * FKS), "s"(fp) : "memory"); \
    if (__all(f[0] == tag)) break;                                                                                        \
    if (KSL) __builtin_amdgcn_s_sleep(KSL);                                                                               \
  }
#define NRM(i) (__uint_as_float(ct[i]) + __uint_as_float(ct[37 + 2 * i]) * __uint_as_float(ct[37 + 2 * i]) + __uint_as_float(ct[38 + 2 * i]) * __uint_as_float(ct[38 + 2 * i]))
#define FLAG(P) if (kh && (P) == FLP) { asm volatile("s_waitcnt vmcnt(0)" ::: "memory"); \
    if (l == 0 && atomicAdd(ct + 2, 1u) == 7u) st8<WT_>(rw, O_F + (head * 8 + qt) * FKS, u2{tag, ct[36] - 0x30000000u < 0x20000000u ? __float_as_uint(NRM(1)) : 0x7f800000u}); }
  FLAG(0)
  TSTAMP(1);
  if (kq == 2) __builtin_amdgcn_s_setprio(1); else if (kq == 3) __builtin_amdgcn_s_setprio(0);
  const u4 qm[2] = {*(const LDS u4*)qp, *(const LDS u4*)(qp + 512)};
  const u2 qt0[2] = {tail(q0), tail(q0 + 64)}, qt1[2] = {tail(q1), tail(q1 + 64)};
  const f16v z = {0};
  f4 cs[5];
  volatile LDS f4* pm, *p3;
  volatile LDS unsigned* const gf = (volatile LDS unsigned*)ct + 4, *const df = gf + 40;
#define HINT() { unsigned t0, t1; if (kq == 3) __builtin_amdgcn_s_setprio(3); asm volatile("v_mov_b32 %0, %2\n\tv_mov_b32 %1, %3\n\ts_mov_b64 exec, %4\n\tds_write_b32 %0, %0\n\tbuffer_store_dword %1, off, %5, %6 sc1\n\ts_mov_b64 exec, -1" \
    : "=&v"(t0), "=&v"(t1) : "s"(df + w), "s"(tag), "s"((unsigned long long)(kq & kq >> 1)), "s"(rw), "s"((unsigned)O_H + (((L * 8 + qt) * 4 + qg) * 32 + head) * HS) : "memory"); }
#define CHAIN_RD() LANES(); pm = (volatile LDS f4*)(LDS char*)(smem + qg * 4096) + l; p3 = (volatile LDS f4*)(LDS char*)(smem + L_P3 + qg * 1024) + l; \
  cs[4] = *p3; cs[0] = pm[0]; cs[1] = pm[64]; cs[2] = pm[128]; cs[3] = pm[192]
  {
    u2 kt;
    u4 km, v0, v1, p0, p1;
    f16v sa, sb;
#define FETCH5(sl) KV_FETCH(sl, 0); KV_FETCH(sl, 1); KV_FETCH(sl, 2); KV_FETCH(sl, 3); KV_FETCH(sl, 4)
    ATT_PRO(0);
    ATT_LO(0, 0, FLAG(2), FLAG(3))
    ATT_HI(0, 0, FLAG(4), NOP)
    if (w < 4) {
      u2 f;
      POLL(f, qt ^ (1 + (l & 1)))
      TSTAMP(1);
      FETCH5(1); FETCH5(2);
      asm volatile("s_waitcnt vmcnt(0)" ::: "memory");
    }
    STAMP(4);
    BAR("");
    STAMP(5);
    if (KP0 != 3) { if (kq == 0) __builtin_amdgcn_s_setprio(3); else if (kq == 1) __builtin_amdgcn_s_setprio(2); }
    CYC0();
    if (w < 4) {
      u2 f;
      POLL(f, l & 7)
      TSTAMP(2);
      FETCH5(3); FETCH5(4);
      const bool ok = __all(__uint_as_float(f[1]) * NRM(0) * 1.02f <= ATT_BOUND * ATT_BOUND);
      if (tid == 0) ct[3] = ok;
    } else if (kq == OPW) {
      const unsigned o0 = O_O + (qt * 8 + qg * 2) * OTB + head * 1024 + lo16;
      st16<WT_>(rw, o0, ONES); st16<WT_>(rw, o0 + OTB, ONES);
      if (!L && b < 144) {
#pragma unroll
        for (int i = 0; i < 4; i++) st16<WT_>(rw, lo16 + i * 1024, ONES, O_XT3 + b * S * 8 + qg * 4096);
      }
      st4<WT_>(rw, O_O + (qt * 8 + qg * 2 + (lo16 >> 9)) * OTB + 32768 + head * 128 + ((lo16 >> 2) & 127), ~0u);
      if (OTC && L) {
        const int r0 = qt * 256 + (head >> 2) * 32;
        touch(smem, mkrs(out, ACT), ((lo16 >> 4 & 15) + (lo16 & 256)) * 1152 + (lo16 >> 9) * 576, ((r0 & ~511) + ((r0 & 511) >> 1)) * 1152 + (head & 3) * 144 + min(qg * 48, 140));
      }
    }
    ATT_PRO(1);
    ATT_LO(1, 1, NOP, NOP)
    ATT_HI(1, 2, NOP, NOP)
    ATT_LO(2, 1, NOP, NOP)
    BAR("s_waitcnt vmcnt(0) lgkmcnt(0)\n\t");
    if (kq) { FETCH5(4 + kq); }
    if (__builtin_amdgcn_readfirstlane(ct[3])) {
      ATT_HI(2, 3, NOP, NOP)
      ATT_ADV(3);
      ATT_LO(0, 1, NOP, NOP)
      ATT_HI(0, 1, NOP, NOP)
      ATT_LO(1, 1, NOP, NOP)
      BAR("s_waitcnt vmcnt(0)\n\t");
      ATT_HI(1, 2, NOP, NOP)
      ATT_ADV(2);
      ATT_LO(0, 1, NOP, NOP)
      ATT_HI(0, 1, NOP, NOP)
      ATT_LO(1, 1, NOP, NOP)
      ATT_HI(1, 2, NOP, NOP)
      ATT_LO(2, 1, NOP, NOP)
      SLOT_A(qt1[1], 2 * CHB + 6 * VP, 2 * CHB, NOP, NOP, 1)
      CHAIN_RD();
      if (HP == 1) { HINT(); }
      SB();
      ATT_E(p0, 0, sb, 0);
      oa = mfma32(v1, p1, oa); PIN(oa);
      ATT_E(p0, 2, sb, 4);
      ob = mfma32(v0, p0, ob); PIN(ob);
      if (HP == 2) { HINT(); }
      ATT_E(p1, 0, sb, 8);
      ATT_E(p1, 2, sb, 12);
      ob = mfma32(v1, p1, ob);
      if (w >= 8 && TAIL != 1) { CYC1(1); }
    } else {
      BAR("s_waitcnt vmcnt(0)\n\t");
      for (int n = 0; n < 2; n++) {
        const u4 q = n ? qm[1] : qm[0];
        const u2 t0 = n ? qt0[1] : qt0[0], t1 = n ? qt1[1] : qt1[0];
        float m = -INFINITY;
        for (int tt = 0; tt < 64; tt++) {
          const char* cb = smem + (tt >> 3) * CHB;
          kt = tail((LDS char*)cb + toff + ((tt >> 1) & 3) * 8 * VP);
          f16v sc = mfma32x8(kt, (tt & 1) ? t1 : t0, z);
          sc = mfma32(*(const u4*)(cb + koff + (tt & 7) * 512), q, sc);
#pragma unroll
          for (int i = 0; i < 16; i++) m = fmaxf(m, sc[i]);
        }
        { f2 e = swap32(m); m = fmaxf(e[0], e[1]); }
        f16v o = {0};
        for (int u = 0; u < 16; u++) {
          const char* cb = smem + (u >> 1) * CHB;
          const int tt = 2 * kq + (u & 1);
          kt = tail((LDS char*)cb + toff + kq * 8 * VP);
          f16v sc = mfma32x8(kt, (u & 1) ? t1 : t0, z);
          sc = mfma32(*(const u4*)(cb + koff + tt * 512), q, sc);
#pragma unroll
          for (int i = 0; i < 4; i++) {
            p0[i] = pkbf(EXP2(sc[2 * i] - m), EXP2(sc[2 * i + 1] - m));
            p1[i] = pkbf(EXP2(sc[8 + 2 * i] - m), EXP2(sc[9 + 2 * i] - m));
          }
          o = mfma32(*(const u4*)(cb + voff + tt * 4 * VP), p0, o);
          o = mfma32(*(const u4*)(cb + voff + (tt * 4 + 2) * VP), p1, o);
        }
        if (n) ob = o; else oa = o;
      }
      BAR("");
      CHAIN_RD();
      if (HP < 3) { HINT(); }
    }
  }
  asm volatile("" : "+v"(oa), "+v"(ob));
  STAMP(6);
  constexpr int L_B = CHB + 36864;
  const int row0 = qt * 256 + (head >> 2) * 32, nt = head & 3;
  {
    f4 t3;
    if (HP == 3) { HINT(); }
#pragma unroll
    for (int i = 0; i < 3; i++) { f2 e = builtin_swap32(oa[8 + i], ob[8 + i]); t3[i] = e[0]; }
    if (kq) {
#pragma unroll 1
      for (int it = 0; it < SPIN && !__all(__float_as_uint(cs[4][3]) == (unsigned)kq); it++) {
        __builtin_amdgcn_s_sleep(2);
        cs[4] = *p3; cs[0] = pm[0]; cs[1] = pm[64]; cs[2] = pm[128]; cs[3] = pm[192];
      }
      t3 += cs[4];
#pragma unroll
      for (int j = 0; j < 8; j++) { oa[j] += cs[j >> 2][j & 3]; ob[j] += cs[2 + (j >> 2)][j & 3]; }
    }
    if (kq < 3) {
#pragma unroll
      for (int i = 0; i < 2; i++) {
        pm[i * 64] = f4{oa[4 * i], oa[4 * i + 1], oa[4 * i + 2], oa[4 * i + 3]};
        pm[128 + i * 64] = f4{ob[4 * i], ob[4 * i + 1], ob[4 * i + 2], ob[4 * i + 3]};
      }
      t3[3] = __uint_as_float(kq + 1);
      *p3 = t3;
    } else {
      const float inv = __builtin_amdgcn_rcpf(t3[2]);
      const f2 e = swap32(inv);
      const unsigned o0 = O_O + (qt * 8 + qg * 2) * OTB + head * 1024 + lo16;
      u4 x, y;
#pragma unroll
      for (int i = 0; i < 4; i++) x[i] = min(pkbf(oa[2 * i] * e[0], oa[2 * i + 1] * e[0]), 0xfffffffeu);
      st16<WT_>(rw, o0, x);
#pragma unroll
      for (int i = 0; i < 4; i++) y[i] = min(pkbf(ob[2 * i] * e[1], ob[2 * i + 1] * e[1]), 0xfffffffeu);
      st16<WT_>(rw, o0 + OTB, y);
      st4<WT_>(rw, O_O + (qt * 8 + qg * 2 + hh) * OTB + 32768 + head * 128 + r * 4, min(pkbf(t3[0] * inv, t3[1] * inv), 0xfffffffeu));
      if (HP == 4) { HINT(); }
      GSTAMP(0);
      STAMP_OUT(3 + 4 * L, 12);
    }
  }
  if (!L && w < 4) fetch_w<16>(smem, ws, head, qg, l);
#define DEAD(SL) _Pragma("unroll 1") for (int it = 0; it < SPIN && !__all(df[l & 15] != 0u); it++) __builtin_amdgcn_s_sleep(SL); asm volatile("" ::: "memory")
  if (w > 8) {
    LANES();
    const int own = head >> 3, j = kq == 3 ? qg : own ^ qg;
    if (kq == 2) df[w] = 1u;
    if (kq == 2 || qg == own) {
      unsigned la = lb + L_O + j * 8192 + lo16, lt = lb + L_O + 32768 + j * 1024 + g * 256 + (l & 3) * 64 + (l & 4) * 2 + (l & 8) / 2, n = SPIN, t;
      volatile LDS unsigned* lf = gf + j, *ld = df + lr;
      asm volatile("" : "+v"(la), "+v"(lt), "+v"(lf), "+v"(ld));
      __builtin_amdgcn_s_setprio(3);
      GSTAMP(0);
      wait_flags<HSL>(rw, O_H + (((L * 8 + qt) * 4 + own) * 32 + 8 * j + (l & 7)) * HS, tag);
      if (HD) __builtin_amdgcn_s_sleep(HD);
#define GM
      asm volatile(GAT_ASM : [n] "+s"(n), [t] "=&s"(t) GM
                   : [vo] "v"(lo16), [so] "s"(O_O + (row0 >> 5) * OTB + j * 8192), [st] "s"(O_O + (row0 >> 5) * OTB + 32768 + j * 1024), [la] "v"(la), [lt] "v"(lt), [lf] "v"(lf), [ld] "v"(ld), [rs] "s"(rw)
                   : "vcc", "scc", "memory", GAT_CLOB);
    }
    if (!L) {
      l = lane_id();
      __builtin_amdgcn_s_setprio(0);
      const int e = w == 12 + (own ^ 1) ? M3P : kq == 3 && qg != own ? CIP : 0;
      if (e) { DEAD(1); }
#pragma unroll 1
      for (int it = 0; it < SPIN && e >= 0 && !__all(gf[e + (l & 3)] != 0u); it++) __builtin_amdgcn_s_sleep(1);
      if (w == 12 + (own ^ 1)) {
        if (l < R1) st8<WT_>(rw, O_M3 + 32768 + (l * 256 + b) * S1, u2{tag, now()});
        STAMP_OUT(9, w);
      }
    }
    return;
  }
  const int j0 = nt * 144 + w * 16;
  f4 acc[2];
  u4 wf[18];
  const unsigned wp = (g * 576 + j0 + lr) * 16;
#define bp ((f4*)(smem + L_B) + l)
  if (!L) {
    const unsigned o = (((j0 >> 2) + g) * S + row0 + 2 * lr) * 8;
    const u4 xr = ld16<WT_>(rw, o, O_XT1);
    st16<WT_>(rw, O_XT2 + o, ONES);
    if (tid < 32) st2<WT_>(rw, O_SS1 + ((row0 + l) * 4 + nt) * 2, 0xffff);
    acc[0] = f4{bflo(xr[0]), bfhi(xr[0]), bflo(xr[1]), bfhi(xr[1])}; acc[1] = f4{bflo(xr[2]), bfhi(xr[2]), bflo(xr[3]), bfhi(xr[3])};
  } else if (w < 8 && !(NOX & 1)) {
    volatile LDS unsigned* fl = (volatile LDS unsigned*)ct + 8;
    const rsrc_t r0 = mkrs(ws + O_RT, WSQ);
    if (XPR >= 0) __builtin_amdgcn_s_setprio(XPR);
    if (w < 4) {
#pragma unroll
      for (int t = 0; t < 9; t++) wf[t] = ld16<WT_>(r0, wp, t * 4 * 576 * 16);
      unsigned so = O_XT3 + w * 36 * S * 8;
      asm volatile(STG_ASM : [so] "+s"(so) : [vo] "v"((g * S + row0 + 2 * lr) * 8), [la] "v"(lb + CHB + w * 9216 + lo16), [rs] "s"(rw) : "scc", "memory", GAT_CLOB);
      fl[w] = 1u;
#pragma unroll
      for (int t = 9; t < 18; t++) wf[t] = ld16<WT_>(r0, wp, t * 4 * 576 * 16);
    } else {
#pragma unroll
      for (int t = 0; t < 5; t++) wf[t] = ld16<WT_>(r0, wp, (qg + 4 * t) * 4 * 576 * 16 + (8 - w) * 256);
    }
#pragma unroll 1
    for (int it = 0; it < SPIN && !__all(fl[l & 3] != 0u); it++) __builtin_amdgcn_s_sleep(2);
    asm volatile("" ::: "memory");
    const LDS char* xa = (LDS char*)smem + CHB + g * 512 + lr * 16 + (w < 4 ? 0 : qg * 2048);
#define XRD(n, k) *(const LDS u4*)(xa + (k) * 2048 + (n) * 256)
    const f4 z4 = {0, 0, 0, 0};
    if (XPQ >= 0) __builtin_amdgcn_s_setprio(XPQ);
    if (NOX & 2) {
#pragma unroll
      for (int t = 0; t < 18; t++) asm volatile("" :: "v"(wf[t]));
    } else if (w < 4) {
      f4 od[2];
      u4 xf[18][2];
#pragma unroll
      for (int t = 0; t < 3; t++) { xf[t][0] = XRD(0, t); xf[t][1] = XRD(1, t); }
      SB();
#pragma unroll
      for (int t = 0; t < 18; t++)
#pragma unroll
        for (int n = 0; n < 2; n++) {
          if (t + 3 < 18) xf[t + 3][n] = XRD(n, t + 3);
          f4& s = t < 9 ? acc[n] : od[n];
          s = mfma16(wf[t], xf[t][n], t % 9 ? s : z4);
          PIN(s);
        }
#pragma unroll
      for (int n = 0; n < 2; n++) { bp[w * 128 + n * 64] = acc[n] - od[n]; acc[n] += od[n]; }
    } else {
#pragma unroll
      for (int t = 0; t < 5; t++) { wf[5 + 2 * t] = XRD(0, 4 * t); wf[6 + 2 * t] = XRD(1, 4 * t); }
      SB();
#pragma unroll
      for (int t = 0; t < 4; t++)
#pragma unroll
        for (int n = 0; n < 2; n++) acc[n] = mfma16(wf[t], wf[5 + 2 * t + n], t ? acc[n] : z4);
      if (w < 6) { acc[0] = mfma16(wf[4], wf[13], acc[0]); acc[1] = mfma16(wf[4], wf[14], acc[1]); }
      bp[w * 128] = acc[0]; bp[w * 128 + 64] = acc[1];
    }
    asm volatile("" : "+v"(acc[0]), "+v"(acc[1]) :: "memory");
  }
  LANES();
  const rsrc_t r1 = mkrs(ws + (L ? O_WOT : O_WO2), WSQ);
#pragma unroll
  for (int t = 0; t < 16; t++) wf[t] = ld16<WT_>(r1, wp, t * 4 * 576 * 16);
  u2 wt2[4];
#pragma unroll
  for (int t = 0; t < 4; t++) wt2[t] = ld8<WT_>(r1, ((g >> 1) * 576 + j0 + lr) * 16 + (g & 1) * 8, (64 + 2 * t) * 576 * 16);
  const LDS char* xp = (LDS char*)smem + L_O + g * 512 + lr * 16, *xt = (LDS char*)smem + L_O + lo16;
  __builtin_amdgcn_s_waitcnt(0x70);
  __builtin_amdgcn_s_setprio(0);
  USTAMP(1);
  df[w] = 1u;
  DEAD(FSL);
  USTAMP(2);
  if (L && w >= 4) {
    acc[0] = bp[(w - 4) * 128]; acc[1] = bp[(w - 4) * 128 + 64];
    if (w == 8) {
#pragma unroll
      for (int i = 5; i < 8; i++) { acc[0] += bp[i * 128]; acc[1] += bp[i * 128 + 64]; }
    }
  }
  volatile LDS unsigned* gp = gf + (l & 3);
  unsigned o1 = L ? (((row0 & ~511) + ((row0 & 511) >> 1) + 256 * (g & 1) + lr) * D + 72 * nt + (w < 8 ? (w >> 2) * 288 + qg * 16 + 8 * (g >> 1) : 64 + 288 * (g >> 1))) * 2
                  : O_XT2 + (((j0 >> 2) + g) * S + row0 + 2 * lr) * 8, o2 = O_SS1 + ((row0 + 2 * lr + g) * 4 + nt) * 2;
  asm volatile("" : "+v"(acc[0]), "+v"(acc[1]), "+v"(xp), "+v"(xt), "+v"(gp), "+v"(o1), "+v"(o2));
  {
    unsigned todo = 15;
#define GRP(j) if (m >> (j) & 1) {                                                                                       \
      u4 xf[4][2]; f4 s[2];                                                                                              \
      const u4 yf = *(const LDS u4*)(xt + 32768 + (j) * 1024);                                                           \
      _Pragma("unroll") for (int t = 0; t < 8; t++) xf[t >> 1][t & 1] = *(const LDS u4*)(xp + (4 * (j) + (t >> 1)) * 2048 + (t & 1) * 256); \
      SB();                                                                                                              \
      _Pragma("unroll") for (int n = 0; n < 2; n++) { s[n] = mfma16x16(wt2[j], u2{yf[2 * n], yf[2 * n + 1]}, (j) ? f4{0, 0, 0, 0} : acc[n]); PIN(s[n]); } \
      _Pragma("unroll") for (int t = 0; t < 8; t++) { s[t & 1] = mfma16(wf[4 * (j) + (t >> 1)], xf[t >> 1][t & 1], s[t & 1]); PIN(s[t & 1]); } \
      if (j) { wf[4 * (j)] = __builtin_bit_cast(u4, s[0]); wf[4 * (j) + 1] = __builtin_bit_cast(u4, s[1]); } else { acc[0] = s[0]; acc[1] = s[1]; } \
    }
#pragma unroll 1
    for (int it = 0; it < SPIN && todo; it++) {
      const unsigned m = (unsigned)__ballot(*gp != 0u) & todo;
      asm volatile("" ::: "memory");
      if (!m) { if (GSP) __builtin_amdgcn_s_sleep(GSP); continue; }
      USTAMP(4);
      todo &= ~m;
      GRP(0) GRP(1) GRP(2) GRP(3)
    }
#pragma unroll
    for (int n = 0; n < 2; n++) acc[n] = (acc[n] + __builtin_bit_cast(f4, wf[4 + n])) + (__builtin_bit_cast(f4, wf[8 + n]) + __builtin_bit_cast(f4, wf[12 + n]));
  }
  USTAMP(5);
  if (L) {
    st16<OUTA>(mkrs(out, ACT), o1, pair16(acc[0], acc[1]));
    if (b + w + lane_id() == 0) st4<OUTA>(rw, O_EP, tag);
    if (WBL) asm volatile("s_waitcnt vmcnt(0)\n\tbuffer_wbl2 sc1" ::: "memory");
  } else {
    st16<WT_>(rw, o1, pub(pack8(acc[0], acc[1])));
    l = lane_id();
    const float q = red16(ssq(acc[0], j0 + (l >> 4)), ssq(acc[1], j0 + (l >> 4)));
    row_stats<32>(smem + 16384, w, l, ct + 12, rw, q, q, o2);
  }
  STAMP_OUT(2 + 4 * L, 0);
  STAMP_OUT(4 + 4 * L, WSTAMP);
}

extern "C" __global__ __launch_bounds__(1024, 4) void k_main(char*, u16*, const u16*, const u16*, const u16*, const u16*, const u16*) {
  __shared__ __attribute__((aligned(16))) char smem[163840];
  Args ka;
  asm volatile("s_load_dwordx8 %0, %3, 0\n\ts_load_dwordx4 %1, %3, 0x20\n\ts_load_dwordx2 %2, %3, 0x30" : "=&s"(ka.a), "=&s"(ka.b), "=&s"(ka.c) : "s"(__builtin_amdgcn_kernarg_segment_ptr()));
  const int w = __builtin_amdgcn_readfirstlane(threadIdx.x >> 6);
  const int b = blockIdx.x & 255, L = blockIdx.x >> 8;
  PAD0();
  unsigned tag;
  asm volatile("v_readlane_b32 %0, %1, 1" : "=s"(tag) : "v"(threadIdx.x));
  if (!L) tag = first(smem, ka, b, w);
  ARGS(ka.a[1], ka.c);
  u16* const out = (u16*)(GLB u16*)p1;
  const GLB u16* const wo = (const GLB u16*)p2;
  bool seen = false;
  {
    const int l = lane_id(), tid = w * 64 + l;
    STAMP_DECL; STAMP(0);
    __builtin_amdgcn_s_setprio(0);
    if (b < 144) {
      seen = colconv(smem + L_X, ws, b, L, w, l, tag TARG);
      STAMP_OUT(1 + L * 4, CVW);

#define WOTB 216
    } else if (L && b < WOTB && w < 9) {
      const int kb = b - 144, nt = tid / 144, u = tid - 144 * nt, d = 72 * nt + (u < 64 ? u : u < 128 ? u + 224 : (u & 7) + 64 + (u & 8) * 36);
      u4 v, r;
#pragma unroll
      for (int i = 0; i < 4; i++) {
        v[i] = (unsigned)wo[fmap(kb * 8 + 2 * i) * D + d] | ((unsigned)wo[fmap(kb * 8 + 2 * i + 1) * D + d] << 16);
        int j = kb * 8 + 2 * i;
        r[i] = (unsigned)RV(CT_DEV, d, j) | ((unsigned)RV(CT_DEV, d, j + 1) << 16);
      }
      st16<WT_>(mkrs(ws, WS_END), O_WOT + (kb * 576 + tid) * 16, v);
      st16<WT_>(mkrs(ws, WS_END), O_RT + (kb * 576 + tid) * 16, r);
    }
  }
  asm volatile("" : "+s"(tag));
  layer(smem, ws, out, b, L, w, tag, seen);
  PAD(4 + 4 * L)
  if (!L) asm volatile("v_writelane_b32 v0, %3, 1\n\ts_branch k_main" :: "{s[0:1]}"(__builtin_amdgcn_kernarg_segment_ptr()), "{s2}"(b | 256), "{v0}"(w * 64), "s"(tag) : "memory");
}

extern "C" void kernel_launch(void* const* d_in, const int* in_sizes, int n_in, void* d_out, int out_size, void* d_ws, size_t ws_size, hipStream_t stream) {
  k_main<<<256, 1024, 0, stream>>>((char*)d_ws, (u16*)d_out, (const u16*)d_in[0], (const u16*)d_in[1], (const u16*)d_in[2], (const u16*)d_in[4], (const u16*)d_in[5]);
}
```
